```python
import torch

def _softmax(x, axis=-1):
    return torch.softmax(x, dim=axis)
def _gelu(x):
    return torch.nn.functional.gelu(x, approximate="tanh")
def _silu(x):
    return torch.nn.functional.silu(x)
def _sigmoid(x):
    return torch.sigmoid(x)
def _layernorm(x, axis=-1, eps=1e-5):
    m = x.mean(dim=axis, keepdim=True)
    v = x.var(dim=axis, keepdim=True, unbiased=False)
    return (x - m) / torch.sqrt(v + eps)
def _scatter_add(tgt, idx, src, axis=0):
    return tgt.clone().index_add_(axis, idx, src)
def _segment_sum(src, seg, n):
    out = torch.zeros((n,) + src.shape[1:], dtype=src.dtype)
    return out.index_add_(0, seg, src)
def _pad(x, pw):
    flat = tuple(v for pair in reversed(pw) for v in pair)
    return torch.nn.functional.pad(x, flat)

kernel_name = 'interleave_layers__ssm_conv_s4d__grouped_kv_mqa__prompt'

def setup_inputs():

    _w = (lambda a: (lambda t: t.to(torch.bfloat16) if t.is_floating_point() else t)(torch.as_tensor(a)))
    def _A_setup():
        Bsz, L, D, N = 1, 2048, 640, 128
        return {
            "u": _w((lambda *s: torch.randn(*s, dtype=torch.bfloat16) * 0.05)(Bsz, L, D)),
            "log_A_real": _w((lambda *s: torch.randn(*s, dtype=torch.bfloat16) * 3.2)(D, N) * 0.5),
            "A_imag": _w((lambda *s: torch.randn(*s, dtype=torch.bfloat16) * 0.05)(D, N) * 3.14),
            "C_real": _w((lambda *s: torch.randn(*s, dtype=torch.bfloat16) * 0.05)(D, N) / N ** 0.5),
            "C_imag": _w((lambda *s: torch.randn(*s, dtype=torch.bfloat16) * 0.05)(D, N) / N ** 0.5),
            "log_dt": _w((lambda *s: torch.randn(*s, dtype=torch.bfloat16) * 0.05)(D) * 0.5 - 4.0),
            "D_skip": _w((lambda *s: torch.randn(*s, dtype=torch.bfloat16) * 0.05)(D)),
        }
    _A_in = _A_setup()

    def _B_setup():
        B, S, D, H = 1, 2048, 640, 32
        Dh = D // H
        return {
            "x": _w((lambda *s: torch.randn(*s, dtype=torch.bfloat16) * 0.05)(B, S, D)),
            "wq": _w((lambda *s: torch.randn(*s, dtype=torch.bfloat16) * 0.05)(D, H * Dh)),
            "wk": _w((lambda *s: torch.randn(*s, dtype=torch.bfloat16) * 0.05)(D, Dh)),
            "wv": _w((lambda *s: torch.randn(*s, dtype=torch.bfloat16) * 0.05)(D, Dh)),
            "wo": _w((lambda *s: torch.randn(*s, dtype=torch.bfloat16) * 0.05)(H * Dh, D)),
        }
    _B_in = _B_setup()

    return {
        "A_u": _A_in["u"],
        "A_log_A_real": _A_in["log_A_real"],
        "A_A_imag": _A_in["A_imag"],
        "A_C_real": _A_in["C_real"],
        "A_C_imag": _A_in["C_imag"],
        "A_log_dt": _A_in["log_dt"],
        "A_D_skip": _A_in["D_skip"],
        "B_x": _B_in["x"],
        "B_wq": _B_in["wq"],
        "B_wk": _B_in["wk"],
        "B_wv": _B_in["wv"],
        "B_wo": _B_in["wo"],
    }

def reference(A_u, A_log_A_real, A_A_imag, A_C_real, A_C_imag, A_log_dt, A_D_skip, B_x, B_wq, B_wk, B_wv, B_wo):
    def _A_ref(u, log_A_real, A_imag, C_real, C_imag, log_dt, D_skip):
        L = u.shape[1]
        dt = torch.exp(log_dt)
        A = -torch.exp(log_A_real) + 1j * A_imag
        C = (C_real + 1j * C_imag) * dt[:, None]
        pos = (lambda x, dt: x.to(dt))(torch.arange(L), log_dt.dtype)
        P = torch.exp((A * dt[:, None])[:, :, None] * pos[None, None, :])
        K = 2.0 * torch.einsum("dn,dnl->dl", C, P).real
        L2 = 2 * L
        u_t = (lambda x, *d: x.permute(*d))(u, 0, 2, 1)
        Kf = (lambda x, *a, **k: torch.fft.rfft(x.float(), *a, **k))(K, L2)
        Uf = (lambda x, *a, **k: torch.fft.rfft(x.float(), *a, **k))(u_t, L2)
        y = (lambda x, *a, **k: torch.fft.irfft(x, *a, **k).to(torch.bfloat16))(Uf * Kf[None, :, :], L2)[:, :, :L]
        y = (lambda x, *d: x.permute(*d))(y, 0, 2, 1)
        return y + u * D_skip[None, None, :]

    def _B_ref(x, wq, wk, wv, wo):
        B, S, D = x.shape
        H = 32
        Dh = D // H
        q = (lambda x, *d: x.permute(*d))((x @ wq).reshape(B, S, H, Dh), 0, 2, 1, 3)
        k = x @ wk
        v = x @ wv
        scores = torch.einsum("bhqd,bkd->bhqk", q, k) * (Dh ** -0.5)
        p = _softmax(scores, axis=-1)
        o = torch.einsum("bhqk,bkd->bhqd", p, v)
        return (lambda x, *d: x.permute(*d))(o, 0, 2, 1, 3).reshape(B, S, H * Dh) @ wo

    x = A_u
    x = x + _A_ref(_layernorm(x, axis=-1), A_log_A_real, A_A_imag, A_C_real, A_C_imag, A_log_dt, A_D_skip)
    x = x + _B_ref(_layernorm(x, axis=-1), B_wq, B_wk, B_wv, B_wo)
    x = x + _A_ref(_layernorm(x, axis=-1), A_log_A_real, A_A_imag, A_C_real, A_C_imag, A_log_dt, A_D_skip)
    x = x + _B_ref(_layernorm(x, axis=-1), B_wq, B_wk, B_wv, B_wo)
    return x
```

```cpp
#include <hip/hip_runtime.h>
#include <hip/hip_bf16.h>
#include <stdint.h>

typedef unsigned short u16;
typedef __attribute__((ext_vector_type(8))) __bf16 bf16x8;
typedef __attribute__((ext_vector_type(8))) _Float16 f16x8;
typedef __attribute__((ext_vector_type(16))) float f32x16;
typedef __attribute__((ext_vector_type(4))) float f32x4;
typedef __attribute__((ext_vector_type(4))) unsigned u32x4;
typedef __attribute__((ext_vector_type(2))) unsigned u32x2;
typedef __attribute__((ext_vector_type(4))) short s16x4;
typedef __attribute__((ext_vector_type(2))) float f32x2;

#define LDS_AS __attribute__((address_space(3)))

constexpr int L_ = 2048, D_ = 640, NS_ = 128;
constexpr int KFROW = 2304;
constexpr float LOG2E = 1.4426950408889634f;
constexpr float INV2PI = 0.15915494309189535f;
constexpr float QSCALE = 0.22360679774997896f * 1.4426950408889634f;

constexpr size_t SZ_ACT = (size_t)D_ * L_ * 2;
constexpr size_t OFF_XT0 = 0;
constexpr size_t OFF_X1T = OFF_XT0 + SZ_ACT;
constexpr size_t OFF_X2T = OFF_X1T + SZ_ACT;
constexpr size_t OFF_X3T = OFF_X2T + SZ_ACT;
constexpr size_t OFF_Q = OFF_X3T + SZ_ACT;
constexpr size_t OFF_OT = OFF_Q + SZ_ACT;
constexpr size_t OFF_KF = OFF_OT + SZ_ACT;
constexpr size_t OFF_KINV = OFF_KF + (size_t)D_ * KFROW * 2;
constexpr size_t OFF_WQKV = OFF_KINV + 4096;
constexpr size_t OFF_WO = OFF_WQKV + (size_t)44 * 20 * 1024;
constexpr size_t OFF_CS = OFF_WO + (size_t)40 * 20 * 1024;
constexpr size_t OFF_KV = OFF_CS + 4096;
constexpr size_t OFF_ST0 = OFF_KV + 262144;
constexpr size_t OFF_ST1 = OFF_ST0 + 65536;

__device__ __forceinline__ float bf2f(u16 v) { return __uint_as_float(((unsigned)v) << 16); }
__device__ __forceinline__ u16 f2bf(float f) { __bf16 b = (__bf16)f; return __builtin_bit_cast(u16, b); }
__device__ __forceinline__ float rbf(float f) { return bf2f(f2bf(f)); }
__device__ __forceinline__ unsigned pk2(float a, float b) { return (unsigned)f2bf(a) | ((unsigned)f2bf(b) << 16); }
__device__ __forceinline__ float lo16(unsigned v) { return __uint_as_float(v << 16); }
__device__ __forceinline__ float hi16(unsigned v) { return __uint_as_float(v & 0xffff0000u); }

constexpr int PREP_KGEN = 640, PREP_LN = 64, PREP_PACK = 84, PREP_KVI = 16;

__device__ __forceinline__ float hi_value(int idx) {
  int v;
  if (idx < 16) v = 128 * (idx >> 3) + (idx & 7);
  else if (idx < 24) v = 256 + 128 * ((idx - 16) >> 2) + 2 * ((idx - 16) & 3);
  else if (idx < 32) v = 512 + 128 * ((idx - 24) >> 1) + 4 * ((idx - 24) & 1);
  else if (idx <= 40) v = 1024 + 128 * (idx - 32);
  else v = 0;
  return (float)v;
}
__device__ __forceinline__ int tab_index(int p) {
  int hi, lo;
  if (p < 256) { hi = 8 * (p >> 7) + (p & 7); lo = (p >> 3) & 15; }
  else if (p < 512) { int r = p - 256; hi = 16 + 4 * (r >> 7) + ((r & 7) >> 1); lo = (r >> 3) & 15; }
  else if (p < 1024) { int r = p - 512; hi = 24 + 2 * (r >> 7) + ((r & 7) >> 2); lo = (r >> 3) & 15; }
  else { int r = p - 1024; hi = 32 + (r >> 7); lo = (r >> 3) & 15; }
  return hi * 16 + lo;
}

__global__ __launch_bounds__(256) void k_prep(const u16* __restrict__ x, const u16* __restrict__ logA,
                                              const u16* __restrict__ Aim, const u16* __restrict__ Cre,
                                              const u16* __restrict__ Cim, const u16* __restrict__ logdt,
                                              const u16* __restrict__ wq, const u16* __restrict__ wk,
                                              const u16* __restrict__ wv, const u16* __restrict__ wo,
                                              char* __restrict__ ws) {
  __shared__ __attribute__((aligned(16))) char smem[51200];
  const int t = threadIdx.x, lane = t & 63, w = t >> 6;
  int b = blockIdx.x;
  if (b < PREP_KGEN) {
    const int d = b;
    float* prm = (float*)smem;
    float* part = prm + 512;
    float* tab = part + 4 * 768;
    float* red = tab + 768;
    const float dt = rbf(expf(bf2f(logdt[d])));
    if (t < 128) {
      float ar = -rbf(expf(bf2f(logA[d * NS_ + t])));
      float ai = bf2f(Aim[d * NS_ + t]);
      float alpha = ar * dt, beta = ai * dt;
      prm[t * 4 + 0] = alpha * LOG2E;
      prm[t * 4 + 1] = beta * INV2PI;
      prm[t * 4 + 2] = 2.0f * (bf2f(Cre[d * NS_ + t]) * dt);
      prm[t * 4 + 3] = 2.0f * (bf2f(Cim[d * NS_ + t]) * dt);
    }
    __syncthreads();
    const int idx = lane & 15;
    const float lo = 8.0f * idx;
    float hv[3] = {hi_value(idx), hi_value(idx + 16), hi_value(idx + 32)};
    f32x4 acc[3] = {{0, 0, 0, 0}, {0, 0, 0, 0}, {0, 0, 0, 0}};
#pragma unroll 2
    for (int g = 0; g < 8; g++) {
      const int n = 4 * (8 * w + g) + (lane >> 4);
      const f32x4 p = *(const f32x4*)(prm + n * 4);
      float e = __builtin_amdgcn_exp2f(p[0] * lo);
      float ph = __builtin_amdgcn_fractf(p[1] * lo);
      const float blr = e * __builtin_amdgcn_cosf(ph), bli = e * __builtin_amdgcn_sinf(ph);
#pragma unroll
      for (int tt = 0; tt < 3; tt++) {
        float e2 = __builtin_amdgcn_exp2f(p[0] * hv[tt]);
        float ph2 = __builtin_amdgcn_fractf(p[1] * hv[tt]);
        float er = e2 * __builtin_amdgcn_cosf(ph2), ei = e2 * __builtin_amdgcn_sinf(ph2);
        float Ar = p[2] * er - p[3] * ei;
        float Ai = p[2] * ei + p[3] * er;
        acc[tt] = __builtin_amdgcn_mfma_f32_16x16x4f32(Ar, blr, acc[tt], 0, 0, 0);
        acc[tt] = __builtin_amdgcn_mfma_f32_16x16x4f32(-Ai, bli, acc[tt], 0, 0, 0);
      }
    }
#pragma unroll
    for (int tt = 0; tt < 3; tt++)
#pragma unroll
      for (int r = 0; r < 4; r++) part[w * 768 + (16 * tt + (lane >> 4) * 4 + r) * 16 + idx] = acc[tt][r];
    __syncthreads();
    for (int i = t; i < 768; i += 256) tab[i] = (part[i] + part[768 + i]) + (part[1536 + i] + part[2304 + i]);
    __syncthreads();
    float kv[8];
    float mx = 0.f;
#pragma unroll
    for (int e = 0; e < 8; e++) {
      int j = 8 * t + e;
      int p = (int)rbf((float)j);
      kv[e] = tab[tab_index(p)];
      mx = fmaxf(mx, fabsf(kv[e]));
    }
#pragma unroll
    for (int o = 32; o > 0; o >>= 1) mx = fmaxf(mx, __shfl_xor(mx, o));
    if (lane == 0) red[w] = mx;
    __syncthreads();
    mx = fmaxf(fmaxf(red[0], red[1]), fmaxf(red[2], red[3]));
    int ex = 0;
    if (mx > 0.f && mx < 3.0e38f) { frexpf(mx, &ex); ex = max(ex, -100); } else ex = 15;
    const float sc = ldexpf(1.0f, 15 - ex);
    f16x8 o;
#pragma unroll
    for (int e = 0; e < 8; e++) o[e] = (_Float16)(kv[e] * sc);
    f16x8* dst = (f16x8*)(ws + OFF_KF + (size_t)d * KFROW * 2);
    dst[31 + t] = o;
    if (t < 32) {
      f16x8 z = {0, 0, 0, 0, 0, 0, 0, 0};
      dst[t < 31 ? t : 287] = z;
    }
    if (t == 0) ((float*)(ws + OFF_KINV))[d] = ldexpf(1.0f, ex - 15 - 9);
    return;
  }
  b -= PREP_KGEN;
  if (b < PREP_LN) {
    const int l0 = b * 32, row = t >> 3, part = t & 7;
    u16* tile = (u16*)smem;
    const u32x4* src = (const u32x4*)(x + (size_t)(l0 + row) * D_ + part * 80);
    u32x4 v[10];
#pragma unroll
    for (int i = 0; i < 10; i++) v[i] = src[i];
    float s = 0.f;
#pragma unroll
    for (int i = 0; i < 10; i++)
#pragma unroll
      for (int j = 0; j < 4; j++) s += lo16(v[i][j]) + hi16(v[i][j]);
    s += __shfl_xor(s, 1);
    const float mean = s * (1.0f / 160.0f);
    float m2 = 0.f;
#pragma unroll
    for (int i = 0; i < 10; i++)
#pragma unroll
      for (int j = 0; j < 4; j++) {
        float a = lo16(v[i][j]) - mean, c = hi16(v[i][j]) - mean;
        m2 += a * a + c * c;
        int dd = part * 80 + i * 8 + j * 2;
        tile[dd * 40 + row] = (u16)(v[i][j] & 0xffffu);
        tile[(dd + 1) * 40 + row] = (u16)(v[i][j] >> 16);
      }
    m2 += __shfl_xor(m2, 1);
    if ((part & 1) == 0) {
      f32x2 o = {mean, m2};
      ((f32x2*)(ws + OFF_ST0))[(part >> 1) * L_ + l0 + row] = o;
    }
    __syncthreads();
    u16* xt = (u16*)(ws + OFF_XT0);
    for (int i = t; i < 640 * 4; i += 256) {
      int dd = i >> 2, c = i & 3;
      *(u32x4*)(xt + (size_t)dd * L_ + l0 + c * 8) = *(const u32x4*)(tile + dd * 40 + c * 8);
    }
    return;
  }
  b -= PREP_LN;
  if (b < PREP_PACK) {
    const bool isq = b < 44;
    const int nt = isq ? b : b - 44;
    const int n = nt * 16 + (lane & 15);
    const u16* src; int ld; bool valid = true;
    if (isq) {
      if (n < 640) { src = wq + n; ld = 640; }
      else if (n < 660) { src = wk + (n - 640); ld = 20; }
      else if (n < 680) { src = wv + (n - 660); ld = 20; }
      else { src = wq; ld = 640; valid = false; }
    } else { src = wo + n; ld = 640; }
    u32x4* dst = (u32x4*)(ws + (isq ? OFF_WQKV : OFF_WO)) + (size_t)nt * 20 * 64;
    float cs = 0.f;
#pragma unroll
    for (int i = 0; i < 5; i++) {
      const int ks = w + 4 * i;
      const int k0 = ks * 32 + (lane >> 4) * 8;
      u16 e[8];
#pragma unroll
      for (int j = 0; j < 8; j++) { e[j] = valid ? src[(size_t)(k0 + j) * ld] : (u16)0; cs += bf2f(e[j]); }
      u32x4 o;
#pragma unroll
      for (int j = 0; j < 4; j++) o[j] = (unsigned)e[2 * j] | ((unsigned)e[2 * j + 1] << 16);
      dst[ks * 64 + lane] = o;
    }
    if (isq) {
      float* red = (float*)smem;
      cs += __shfl_xor(cs, 16);
      cs += __shfl_xor(cs, 32);
      if (lane < 16) red[w * 16 + lane] = cs;
      __syncthreads();
      if (t < 16) ((float*)(ws + OFF_CS))[nt * 16 + t] = (red[t] + red[16 + t]) + (red[32 + t] + red[48 + t]);
    }
    return;
  }
  b -= PREP_PACK;
  {
    u32x4* dst = (u32x4*)(ws + OFF_KV);
#pragma unroll
    for (int i = 0; i < 4; i++) {
      int c = b * 1024 + i * 256 + t;
      int piece = (c >> 6) & 3, ln = c & 63;
      unsigned v = (piece >= 2 && (ln & 31) == 20) ? 0x3F803F80u : 0u;
      u32x4 o = {v, v, v, v};
      dst[c] = o;
    }
  }
}

constexpr int CV_COPY = 321;
constexpr int CV_ZERO = 8 * CV_COPY + 8;
constexpr int CV_K = CV_ZERO + 48;
constexpr int CV_STAGE = CV_K + 288;
constexpr int CV_TOTAL = CV_STAGE + 257;

__global__ __launch_bounds__(256) void k_conv(const u16* __restrict__ xT, const f32x2* __restrict__ st,
                                              const char* __restrict__ ws, const u16* __restrict__ dskip,
                                              u16* __restrict__ outT) {
  __shared__ __attribute__((aligned(16))) u32x4 sm[CV_TOTAL];
  const int t = threadIdx.x, lane = t & 63, w = t >> 6;
  const int d = blockIdx.x;
  const int ci = lane & 31, h = lane >> 5;
  const u32x4 xv = *(const u32x4*)(xT + (size_t)d * L_ + 8 * t);
  const u32x4* kg = (const u32x4*)(ws + OFF_KF + (size_t)d * KFROW * 2);
  const u32x4 k0v = kg[t];
  u32x4 k1v = {0, 0, 0, 0};
  if (t < 32) k1v = kg[256 + t];
  const int lo0 = 8 * ci + 4 * h + 256 * w;
  const u32x2 xo0 = *(const u32x2*)(xT + (size_t)d * L_ + lo0);
  const u32x2 xo1 = *(const u32x2*)(xT + (size_t)d * L_ + lo0 + 1024);
  const float kinv = ((const float*)(ws + OFF_KINV))[d];
  const float dsk = bf2f(dskip[d]);
  f32x4 sp[4][4];
#pragma unroll
  for (int p = 0; p < 4; p++)
#pragma unroll
    for (int i = 0; i < 4; i++) sp[p][i] = *(const f32x4*)(st + p * L_ + 8 * t + 2 * i);
  sm[CV_K + t] = k0v;
  if (t < 32) sm[CV_K + 256 + t] = k1v;
  if (t < 48) { u32x4 z = {0, 0, 0, 0}; sm[CV_ZERO + t] = z; }
  unsigned cw[4];
#pragma unroll
  for (int i = 0; i < 4; i++) {
    float uu[2];
#pragma unroll
    for (int e = 0; e < 2; e++) {
      float m0 = sp[0][i][2 * e], m1 = sp[1][i][2 * e], m2 = sp[2][i][2 * e], m3 = sp[3][i][2 * e];
      float mean = 0.25f * ((m0 + m1) + (m2 + m3));
      float a0 = m0 - mean, a1 = m1 - mean, a2 = m2 - mean, a3 = m3 - mean;
      float M2 = ((sp[0][i][2 * e + 1] + sp[1][i][2 * e + 1]) + (sp[2][i][2 * e + 1] + sp[3][i][2 * e + 1])) +
                 160.0f * ((a0 * a0 + a1 * a1) + (a2 * a2 + a3 * a3));
      float mb = rbf(mean);
      float vb = rbf(M2 * (1.0f / 640.0f));
      float s = rbf(sqrtf(rbf(vb + 1e-5f)));
      float xx = e ? hi16(xv[i]) : lo16(xv[i]);
      uu[e] = rbf(rbf(xx - mb) / s);
    }
    _Float16 f0 = (_Float16)(uu[0] * 512.0f), f1 = (_Float16)(uu[1] * 512.0f);
    cw[i] = (unsigned)__builtin_bit_cast(u16, f0) | ((unsigned)__builtin_bit_cast(u16, f1) << 16);
  }
  {
    u32x4 c4 = {cw[0], cw[1], cw[2], cw[3]};
    sm[CV_STAGE + 1 + t] = c4;
    if (t == 0) { u32x4 z = {0, 0, 0, 0}; sm[CV_STAGE] = z; }
  }
  __syncthreads();
  {
    const u32x4 pv = sm[CV_STAGE + t];
    unsigned Z[8], Y[7];
#pragma unroll
    for (int m = 0; m < 4; m++) {
      Z[m] = __builtin_amdgcn_alignbit(cw[3 - m], cw[3 - m], 16);
      Z[4 + m] = __builtin_amdgcn_alignbit(pv[3 - m], pv[3 - m], 16);
    }
#pragma unroll
    for (int m = 0; m < 7; m++) Y[m] = __builtin_amdgcn_alignbit(Z[m + 1], Z[m], 16);
    const int phys = (t >> 5) * 40 + (t & 31);
#pragma unroll
    for (int r = 0; r < 8; r++) {
      const int o = 7 - r;
      u32x4 v;
      if ((o & 1) == 0) { v[0] = Z[o / 2]; v[1] = Z[o / 2 + 1]; v[2] = Z[o / 2 + 2]; v[3] = Z[o / 2 + 3]; }
      else { v[0] = Y[o / 2]; v[1] = Y[o / 2 + 1]; v[2] = Y[o / 2 + 2]; v[3] = Y[o / 2 + 3]; }
      sm[r * CV_COPY + phys] = v;
    }
  }
  __syncthreads();
  f32x16 T0 = {0}, T1 = {0};
  {
    const int r = ci & 7, al = ci >> 3;
    const int blo = al - w, bhi = al + 4 - w;
    const int ulo = (blo >= 0) ? (r * CV_COPY + 40 * blo + 1 - h) : (CV_ZERO + r + 8 * (blo & 1) + 1 - h);
    const int uhi = r * CV_COPY + 40 * bhi + 1 - h;
    const int ka = CV_K + ci + h + 32 * w;
#pragma unroll
    for (int s = 0; s < 16; s++) {
      const u32x4 a_lo = sm[ulo + 30 - 2 * s];
      const u32x4 a_hi = sm[uhi + 30 - 2 * s];
      const u32x4 b_a = sm[ka + 2 * s];
      const u32x4 b_b = sm[ka + 2 * s + 128];
      T0 = __builtin_amdgcn_mfma_f32_32x32x16_f16(__builtin_bit_cast(f16x8, a_lo), __builtin_bit_cast(f16x8, b_a), T0, 0, 0, 0);
      T1 = __builtin_amdgcn_mfma_f32_32x32x16_f16(__builtin_bit_cast(f16x8, a_hi), __builtin_bit_cast(f16x8, b_a), T1, 0, 0, 0);
      T1 = __builtin_amdgcn_mfma_f32_32x32x16_f16(__builtin_bit_cast(f16x8, a_lo), __builtin_bit_cast(f16x8, b_b), T1, 0, 0, 0);
    }
  }
  __syncthreads();
  f32x4* red = (f32x4*)sm;
#pragma unroll
  for (int g = 0; g < 4; g++) {
    f32x4 a = {T0[4 * g], T0[4 * g + 1], T0[4 * g + 2], T0[4 * g + 3]};
    f32x4 c = {T1[4 * g], T1[4 * g + 1], T1[4 * g + 2], T1[4 * g + 3]};
    red[((w * 2 + 0) * 4 + g) * 64 + lane] = a;
    red[((w * 2 + 1) * 4 + g) * 64 + lane] = c;
  }
  __syncthreads();
#pragma unroll
  for (int T = 0; T < 2; T++) {
    f32x4 y = (red[((0 * 2 + T) * 4 + w) * 64 + lane] + red[((1 * 2 + T) * 4 + w) * 64 + lane]) +
              (red[((2 * 2 + T) * 4 + w) * 64 + lane] + red[((3 * 2 + T) * 4 + w) * 64 + lane]);
    const int l = lo0 + 1024 * T;
    const int g = l >> 3;
    const u32x2 ur = *((const u32x2*)(sm + 7 * CV_COPY + (g >> 5) * 40 + (g & 31)) + (1 - h));
    float uu[4];
    uu[3] = (float)__builtin_bit_cast(_Float16, (u16)(ur[0] & 0xffffu)) * (1.0f / 512.0f);
    uu[2] = (float)__builtin_bit_cast(_Float16, (u16)(ur[0] >> 16)) * (1.0f / 512.0f);
    uu[1] = (float)__builtin_bit_cast(_Float16, (u16)(ur[1] & 0xffffu)) * (1.0f / 512.0f);
    uu[0] = (float)__builtin_bit_cast(_Float16, (u16)(ur[1] >> 16)) * (1.0f / 512.0f);
    const u32x2 xo = T ? xo1 : xo0;
    float xx[4] = {lo16(xo[0]), hi16(xo[0]), lo16(xo[1]), hi16(xo[1])};
    float r[4];
#pragma unroll
    for (int e = 0; e < 4; e++) {
      float z = rbf(rbf(y[e] * kinv) + rbf(uu[e] * dsk));
      r[e] = xx[e] + z;
    }
    u32x2 o = {pk2(r[0], r[1]), pk2(r[2], r[3])};
    *(u32x2*)(outT + (size_t)d * L_ + l) = o;
  }
}

template <int MODE>
__global__ __launch_bounds__(256) void k_gemm(const u16* __restrict__ XT, const u32x4* __restrict__ Wp,
                                              const u16* __restrict__ resT, u16* __restrict__ out,
                                              char* __restrict__ ws, f32x2* __restrict__ stout) {
  constexpr int NT = (MODE == 0) ? 11 : 10;
  __shared__ __attribute__((aligned(16))) char smem[40960 + 512];
  const int t = threadIdx.x, lane = t & 63, w = t >> 6;
  const int b = blockIdx.x;
  const int cg = (b >> 3) & 3, rb = (b & 7) + 8 * (b >> 5);
  const int l0 = rb * 32;
  const int g = lane >> 4, li = lane & 15;
  const int ntw = (w + 8 < NT) ? 3 : 2;
  {
    u32x4 v[10];
#pragma unroll
    for (int i = 0; i < 10; i++) {
      int row = (t >> 2) + 64 * i;
      v[i] = *(const u32x4*)(XT + (size_t)row * L_ + l0 + (t & 3) * 8);
    }
#pragma unroll
    for (int i = 0; i < 10; i++) {
      int row = (t >> 2) + 64 * i;
      *(u32x4*)(smem + row * 64 + (((t & 3) ^ (2 * ((row >> 3) & 1))) * 16)) = v[i];
    }
  }
  const u32x4* wp0 = Wp + (size_t)(cg * NT + w) * 20 * 64 + lane;
  constexpr int PF = 4;
  u32x4 wf[PF][3];
#pragma unroll
  for (int p = 0; p < PF; p++)
#pragma unroll
    for (int j = 0; j < 3; j++)
      if (j < 2 || ntw == 3) wf[p][j] = wp0[(size_t)j * 4 * 20 * 64 + p * 64];
  __syncthreads();
  f32x4 acc[3][2];
#pragma unroll
  for (int j = 0; j < 3; j++) { acc[j][0] = (f32x4){0, 0, 0, 0}; acc[j][1] = (f32x4){0, 0, 0, 0}; }
  f32x4 sq[2] = {{0, 0, 0, 0}, {0, 0, 0, 0}}, sm1[2] = {{0, 0, 0, 0}, {0, 0, 0, 0}};
  const int q4 = li >> 2, p4 = li & 3;
  const unsigned sbase = (unsigned)(size_t)smem;
  unsigned xa[2];
#pragma unroll
  for (int lt = 0; lt < 2; lt++)
    xa[lt] = (8 * g + q4) * 64 + (((2 * lt + (p4 >> 1)) ^ (2 * (g & 1))) * 16) + 8 * (p4 & 1);
  const u32x4 ones = {0x3F803F80u, 0x3F803F80u, 0x3F803F80u, 0x3F803F80u};
#pragma unroll
  for (int ks = 0; ks < 20; ks++) {
    bf16x8 xf[2];
#pragma unroll
    for (int lt = 0; lt < 2; lt++) {
      s16x4 a = __builtin_amdgcn_ds_read_tr16_b64_v4i16((LDS_AS s16x4*)(size_t)(sbase + xa[lt] + ks * 2048));
      s16x4 c = __builtin_amdgcn_ds_read_tr16_b64_v4i16((LDS_AS s16x4*)(size_t)(sbase + xa[lt] + ks * 2048 + 256));
      u32x2 a2 = __builtin_bit_cast(u32x2, a), c2 = __builtin_bit_cast(u32x2, c);
      u32x4 f = {a2[0], a2[1], c2[0], c2[1]};
      xf[lt] = __builtin_bit_cast(bf16x8, f);
    }
#pragma unroll
    for (int j = 0; j < 3; j++) {
      if (j < 2 || ntw == 3) {
        bf16x8 wv = __builtin_bit_cast(bf16x8, wf[ks % PF][j]);
#pragma unroll
        for (int lt = 0; lt < 2; lt++) {
          if (MODE == 1) acc[j][lt] = __builtin_amdgcn_mfma_f32_16x16x32_bf16(xf[lt], wv, acc[j][lt], 0, 0, 0);
          else acc[j][lt] = __builtin_amdgcn_mfma_f32_16x16x32_bf16(wv, xf[lt], acc[j][lt], 0, 0, 0);
        }
        if (ks + PF < 20) wf[ks % PF][j] = wp0[(size_t)j * 4 * 20 * 64 + (ks + PF) * 64];
      }
    }
    if (MODE == 0 && w == 3) {
#pragma unroll
      for (int lt = 0; lt < 2; lt++) {
        sq[lt] = __builtin_amdgcn_mfma_f32_16x16x32_bf16(xf[lt], xf[lt], sq[lt], 0, 0, 0);
        sm1[lt] = __builtin_amdgcn_mfma_f32_16x16x32_bf16(xf[lt], __builtin_bit_cast(bf16x8, ones), sm1[lt], 0, 0, 0);
      }
    }
  }
  if (MODE == 0) {
    f32x2* stl = (f32x2*)(smem + 40960);
    if (w == 3) {
      if (g == (li >> 2)) {
#pragma unroll
        for (int lt = 0; lt < 2; lt++) {
          float ssq = (p4 == 0) ? sq[lt][0] : (p4 == 1) ? sq[lt][1] : (p4 == 2) ? sq[lt][2] : sq[lt][3];
          float ss = (p4 == 0) ? sm1[lt][0] : (p4 == 1) ? sm1[lt][1] : (p4 == 2) ? sm1[lt][2] : sm1[lt][3];
          float mean = ss * (1.0f / 640.0f);
          float var = fmaxf(ssq * (1.0f / 640.0f) - mean * mean, 0.f);
          float s = rbf(sqrtf(rbf(rbf(var) + 1e-5f)));
          f32x2 o = {rbf(mean), 1.0f / s};
          stl[16 * lt + li] = o;
        }
      }
    }
    __syncthreads();
    const float* cs = (const float*)(ws + OFF_CS);
    u16* qo = (u16*)(ws + OFF_Q);
    char* kvimg = ws + OFF_KV;
#pragma unroll
    for (int j = 0; j < 3; j++) {
      if (j < 2 || ntw == 3) {
        const int n0 = (cg * NT + w + 4 * j) * 16 + 4 * g;
        const f32x4 c4 = *(const f32x4*)(cs + n0);
#pragma unroll
        for (int lt = 0; lt < 2; lt++) {
          const f32x2 sv = stl[16 * lt + li];
          const int l = l0 + 16 * lt + li;
          float v[4];
#pragma unroll
          for (int r = 0; r < 4; r++) v[r] = (acc[j][lt][r] - sv[0] * c4[r]) * sv[1];
          if (n0 < 640) {
            u32x2 o = {pk2(v[0] * QSCALE, v[1] * QSCALE), pk2(v[2] * QSCALE, v[3] * QSCALE)};
            *(u32x2*)(qo + (size_t)l * D_ + n0) = o;
          } else if (n0 < 660) {
            const int dh = n0 - 640;
            u32x2 o = {pk2(v[0], v[1]), pk2(v[2], v[3])};
            const int tile = l >> 5, step = dh >> 4, ln = (l & 31) + 32 * ((dh & 15) >> 3);
            *(u32x2*)(kvimg + tile * 4096 + step * 1024 + ln * 16 + (dh & 7) * 2) = o;
          } else if (n0 < 680) {
            const int dv = n0 - 660;
            const int tile = l >> 5, kk = l & 15, step = (l & 31) >> 4;
            const int hh = (kk & 7) >> 2, jj = (kk >> 3) * 4 + (kk & 3);
#pragma unroll
            for (int r = 0; r < 4; r++)
              *(u16*)(kvimg + tile * 4096 + 2048 + step * 1024 + (dv + r + 32 * hh) * 16 + jj * 2) = f2bf(v[r]);
          }
        }
      }
    }
  } else if (MODE == 1) {
    __syncthreads();
    float* stg = (float*)smem;
#pragma unroll
    for (int j = 0; j < 3; j++) {
      if (j < 2 || ntw == 3) {
        const int nl = (w + 4 * j) * 16 + li;
        const int n = cg * 160 + nl;
#pragma unroll
        for (int lt = 0; lt < 2; lt++) {
          const int ll = 16 * lt + 4 * g;
          const u32x2 xr = *(const u32x2*)(resT + (size_t)n * L_ + l0 + ll);
          float r[4];
          r[0] = rbf(lo16(xr[0]) + rbf(acc[j][lt][0]));
          r[1] = rbf(hi16(xr[0]) + rbf(acc[j][lt][1]));
          r[2] = rbf(lo16(xr[1]) + rbf(acc[j][lt][2]));
          r[3] = rbf(hi16(xr[1]) + rbf(acc[j][lt][3]));
          u32x2 o = {pk2(r[0], r[1]), pk2(r[2], r[3])};
          *(u32x2*)(out + (size_t)n * L_ + l0 + ll) = o;
#pragma unroll
          for (int e = 0; e < 4; e++) stg[(ll + e) * 164 + nl] = r[e];
        }
      }
    }
    __syncthreads();
    const int row = t >> 3, part = t & 7;
    float v[20];
    float s = 0.f;
#pragma unroll
    for (int i = 0; i < 5; i++) {
      f32x4 a = *(const f32x4*)(stg + row * 164 + part * 20 + 4 * i);
#pragma unroll
      for (int e = 0; e < 4; e++) { v[4 * i + e] = a[e]; s += a[e]; }
    }
    s += __shfl_xor(s, 1); s += __shfl_xor(s, 2); s += __shfl_xor(s, 4);
    const float mean = s * (1.0f / 160.0f);
    float m2 = 0.f;
#pragma unroll
    for (int i = 0; i < 20; i++) { float a = v[i] - mean; m2 += a * a; }
    m2 += __shfl_xor(m2, 1); m2 += __shfl_xor(m2, 2); m2 += __shfl_xor(m2, 4);
    if (part == 0) { f32x2 o = {mean, m2}; stout[cg * L_ + l0 + row] = o; }
  } else {
#pragma unroll
    for (int j = 0; j < 3; j++) {
      if (j < 2 || ntw == 3) {
        const int n0 = (cg * NT + w + 4 * j) * 16 + 4 * g;
#pragma unroll
        for (int lt = 0; lt < 2; lt++) {
          const int l = l0 + 16 * lt + li;
          float r[4];
#pragma unroll
          for (int e = 0; e < 4; e++) r[e] = bf2f(resT[(size_t)(n0 + e) * L_ + l]) + rbf(acc[j][lt][e]);
          u32x2 o = {pk2(r[0], r[1]), pk2(r[2], r[3])};
          *(u32x2*)(out + (size_t)l * D_ + n0) = o;
        }
      }
    }
  }
}

__device__ __forceinline__ void pack_p(const f32x16& s, bf16x8& p0, bf16x8& p1) {
  u32x4 a, c;
#pragma unroll
  for (int i = 0; i < 4; i++) {
    a[i] = pk2(__builtin_amdgcn_exp2f(s[2 * i]), __builtin_amdgcn_exp2f(s[2 * i + 1]));
    c[i] = pk2(__builtin_amdgcn_exp2f(s[8 + 2 * i]), __builtin_amdgcn_exp2f(s[9 + 2 * i]));
  }
  p0 = __builtin_bit_cast(bf16x8, a);
  p1 = __builtin_bit_cast(bf16x8, c);
}
__device__ __forceinline__ float max16(const f32x16& s) {
  float m = fmaxf(s[0], s[1]);
#pragma unroll
  for (int i = 2; i < 16; i++) m = fmaxf(m, s[i]);
  return m;
}

__global__ __launch_bounds__(512) void k_attn(const char* __restrict__ ws_q, const u32x4* __restrict__ kv,
                                              u16* __restrict__ oT) {
  __shared__ __attribute__((aligned(16))) u32x4 sm[2][2048];
  const int t = threadIdx.x, lane = t & 63, w = t >> 6;
  const int b = blockIdx.x;
  const int pb = b >> 2, head = (b & 3) * 8 + w;
  const int ci = lane & 31, h = lane >> 5;
  const int l = pb * 32 + ci;
  u32x4 stg[4];
#pragma unroll
  for (int i = 0; i < 4; i++) stg[i] = kv[i * 512 + t];
  bf16x8 q0, q1;
  {
    const char* qp = ws_q + (size_t)l * (D_ * 2) + head * 40;
    u32x2 a = *(const u32x2*)(qp + 16 * h), c = *(const u32x2*)(qp + 16 * h + 8);
    u32x2 e = *(const u32x2*)(qp + 32);
    u32x4 f0 = {a[0], a[1], c[0], c[1]};
    u32x4 f1 = {h ? 0u : e[0], h ? 0u : e[1], 0u, 0u};
    q0 = __builtin_bit_cast(bf16x8, f0);
    q1 = __builtin_bit_cast(bf16x8, f1);
  }
#pragma unroll
  for (int i = 0; i < 4; i++) sm[0][i * 512 + t] = stg[i];
  __syncthreads();
  f32x16 negm;
  {
    f32x16 z = {0};
    f32x16 s = __builtin_amdgcn_mfma_f32_32x32x16_bf16(__builtin_bit_cast(bf16x8, sm[0][lane]), q0, z, 0, 0, 0);
    s = __builtin_amdgcn_mfma_f32_32x32x16_bf16(__builtin_bit_cast(bf16x8, sm[0][64 + lane]), q1, s, 0, 0, 0);
    float m = max16(s);
    m = fmaxf(m, __shfl_xor(m, 32));
#pragma unroll
    for (int i = 0; i < 16; i++) negm[i] = -m;
  }
  f32x16 o = {0};
  for (int c = 0; c < 8; c++) {
    const u32x4* buf = sm[c & 1];
    if (c + 1 < 8) {
#pragma unroll
      for (int i = 0; i < 4; i++) stg[i] = kv[(c + 1) * 2048 + i * 512 + t];
    }
    f32x16 s = __builtin_amdgcn_mfma_f32_32x32x16_bf16(__builtin_bit_cast(bf16x8, buf[lane]), q0, negm, 0, 0, 0);
    s = __builtin_amdgcn_mfma_f32_32x32x16_bf16(__builtin_bit_cast(bf16x8, buf[64 + lane]), q1, s, 0, 0, 0);
#pragma unroll
    for (int tl = 0; tl < 8; tl++) {
      f32x16 sn;
      if (tl + 1 < 8) {
        sn = __builtin_amdgcn_mfma_f32_32x32x16_bf16(__builtin_bit_cast(bf16x8, buf[(tl + 1) * 256 + lane]), q0, negm, 0, 0, 0);
        sn = __builtin_amdgcn_mfma_f32_32x32x16_bf16(__builtin_bit_cast(bf16x8, buf[(tl + 1) * 256 + 64 + lane]), q1, sn, 0, 0, 0);
      }
      bf16x8 p0, p1;
      pack_p(s, p0, p1);
      o = __builtin_amdgcn_mfma_f32_32x32x16_bf16(__builtin_bit_cast(bf16x8, buf[tl * 256 + 128 + lane]), p0, o, 0, 0, 0);
      o = __builtin_amdgcn_mfma_f32_32x32x16_bf16(__builtin_bit_cast(bf16x8, buf[tl * 256 + 192 + lane]), p1, o, 0, 0, 0);
      if (tl + 1 < 8) s = sn;
    }
    if (c + 1 < 8) {
#pragma unroll
      for (int i = 0; i < 4; i++) sm[(c + 1) & 1][i * 512 + t] = stg[i];
    }
    __syncthreads();
  }
  float lsum = __shfl(o[8], ci + 32);
  bool bad = !(lsum <= 1.8446744e19f);
#pragma unroll
  for (int i = 0; i < 12; i++) bad = bad || !(fabsf(o[i]) <= 3.0e38f);
  if (__any(bad)) {
    float m = -INFINITY;
#pragma unroll
    for (int i = 0; i < 16; i++) o[i] = 0.f;
    for (int tl = 0; tl < 64; tl++) {
      f32x16 z = {0};
      f32x16 s = __builtin_amdgcn_mfma_f32_32x32x16_bf16(__builtin_bit_cast(bf16x8, kv[tl * 256 + lane]), q0, z, 0, 0, 0);
      s = __builtin_amdgcn_mfma_f32_32x32x16_bf16(__builtin_bit_cast(bf16x8, kv[tl * 256 + 64 + lane]), q1, s, 0, 0, 0);
      float tm = max16(s);
      tm = fmaxf(tm, __shfl_xor(tm, 32));
      const float mn = fmaxf(m, tm);
      const float al = __builtin_amdgcn_exp2f(m - mn);
      m = mn;
#pragma unroll
      for (int i = 0; i < 16; i++) { o[i] *= al; s[i] -= mn; }
      bf16x8 p0, p1;
      pack_p(s, p0, p1);
      o = __builtin_amdgcn_mfma_f32_32x32x16_bf16(__builtin_bit_cast(bf16x8, kv[tl * 256 + 128 + lane]), p0, o, 0, 0, 0);
      o = __builtin_amdgcn_mfma_f32_32x32x16_bf16(__builtin_bit_cast(bf16x8, kv[tl * 256 + 192 + lane]), p1, o, 0, 0, 0);
    }
    lsum = __shfl(o[8], ci + 32);
  }
  const float inv = 1.0f / lsum;
  u16* op = oT + (size_t)(head * 20 + 4 * h) * L_ + l;
#pragma unroll
  for (int i = 0; i < 12; i++) {
    const int dd = (i & 3) + 8 * (i >> 2);
    if (i < 8 || h == 0) op[(size_t)dd * L_] = f2bf(o[i] * inv);
  }
}

extern "C" void kernel_launch(void* const* d_in, const int* in_sizes, int n_in, void* d_out, int out_size,
                              void* d_ws, size_t ws_size, hipStream_t stream) {
  const u16* x = (const u16*)d_in[0];
  const u16* logA = (const u16*)d_in[1];
  const u16* Aim = (const u16*)d_in[2];
  const u16* Cre = (const u16*)d_in[3];
  const u16* Cim = (const u16*)d_in[4];
  const u16* logdt = (const u16*)d_in[5];
  const u16* dskip = (const u16*)d_in[6];
  const u16* wq = (const u16*)d_in[8];
  const u16* wk = (const u16*)d_in[9];
  const u16* wv = (const u16*)d_in[10];
  const u16* wo = (const u16*)d_in[11];
  char* ws = (char*)d_ws;
  u16* xt0 = (u16*)(ws + OFF_XT0);
  u16* x1t = (u16*)(ws + OFF_X1T);
  u16* x2t = (u16*)(ws + OFF_X2T);
  u16* x3t = (u16*)(ws + OFF_X3T);
  u16* ot = (u16*)(ws + OFF_OT);
  const u32x4* wqkv = (const u32x4*)(ws + OFF_WQKV);
  const u32x4* wop = (const u32x4*)(ws + OFF_WO);
  const u32x4* kv = (const u32x4*)(ws + OFF_KV);
  f32x2* st0 = (f32x2*)(ws + OFF_ST0);
  f32x2* st1 = (f32x2*)(ws + OFF_ST1);

  k_prep<<<PREP_KGEN + PREP_LN + PREP_PACK + PREP_KVI, 256, 0, stream>>>(x, logA, Aim, Cre, Cim, logdt, wq, wk, wv, wo, ws);
  k_conv<<<640, 256, 0, stream>>>(xt0, st0, ws, dskip, x1t);
  k_gemm<0><<<256, 256, 0, stream>>>(x1t, wqkv, nullptr, nullptr, ws, nullptr);
  k_attn<<<256, 512, 0, stream>>>(ws + OFF_Q, kv, ot);
  k_gemm<1><<<256, 256, 0, stream>>>(ot, wop, x1t, x2t, ws, st1);
  k_conv<<<640, 256, 0, stream>>>(x2t, st1, ws, dskip, x3t);
  k_gemm<0><<<256, 256, 0, stream>>>(x3t, wqkv, nullptr, nullptr, ws, nullptr);
  k_attn<<<256, 512, 0, stream>>>(ws + OFF_Q, kv, ot);
  k_gemm<2><<<256, 256, 0, stream>>>(ot, wop, x3t, (u16*)d_out, ws, nullptr);
}
```

```cpp
#include <hip/hip_runtime.h>
#include <hip/hip_bf16.h>
#include <stdint.h>

typedef unsigned short u16;
typedef __attribute__((ext_vector_type(8))) __bf16 bf16x8;
typedef __attribute__((ext_vector_type(8))) _Float16 f16x8;
typedef __attribute__((ext_vector_type(16))) float f32x16;
typedef __attribute__((ext_vector_type(4))) float f32x4;
typedef __attribute__((ext_vector_type(4))) unsigned u32x4;
typedef __attribute__((ext_vector_type(2))) unsigned u32x2;
typedef __attribute__((ext_vector_type(4))) short s16x4;
typedef __attribute__((ext_vector_type(2))) float f32x2;

#define LDS_AS __attribute__((address_space(3)))
#define STAMP(wsp, kid, i)
#define STAMPC(wsp, kid, i)

constexpr int L_ = 2048, D_ = 640, NS_ = 128;
constexpr int KFROW = 2304;
constexpr float LOG2E = 1.4426950408889634f;
constexpr float INV2PI = 0.15915494309189535f;
constexpr float QSCALE = 0.22360679774997896f * 1.4426950408889634f;

constexpr size_t SZ_ACT = (size_t)D_ * L_ * 2;
constexpr size_t OFF_XT0 = 0;
constexpr size_t OFF_X1T = OFF_XT0 + SZ_ACT;
constexpr size_t OFF_X2T = OFF_X1T + SZ_ACT;
constexpr size_t OFF_X3T = OFF_X2T + SZ_ACT;
constexpr size_t OFF_Q = OFF_X3T + SZ_ACT;
constexpr size_t OFF_OT = OFF_Q + SZ_ACT;
constexpr size_t OFF_KF = OFF_OT + SZ_ACT;
constexpr size_t OFF_KINV = OFF_KF + (size_t)D_ * KFROW * 2;
constexpr size_t OFF_WQKV = OFF_KINV + 4096;
constexpr size_t OFF_WO = OFF_WQKV + (size_t)44 * 20 * 1024;
constexpr size_t OFF_CS = OFF_WO + (size_t)40 * 20 * 1024;
constexpr size_t OFF_KV = OFF_CS + 4096;
constexpr size_t OFF_ST0 = OFF_KV + 262144;
constexpr size_t OFF_ST1 = OFF_ST0 + 65536;

__device__ __forceinline__ float bf2f(u16 v) { return __uint_as_float(((unsigned)v) << 16); }
__device__ __forceinline__ u16 f2bf(float f) { __bf16 b = (__bf16)f; return __builtin_bit_cast(u16, b); }
__device__ __forceinline__ float rbf(float f) { return bf2f(f2bf(f)); }
typedef __attribute__((ext_vector_type(2))) __bf16 bf16x2;
__device__ __forceinline__ unsigned pk2(float a, float b) {
  f32x2 v = {a, b};
  return __builtin_bit_cast(unsigned, __builtin_convertvector(v, bf16x2));
}
__device__ __forceinline__ float lo16(unsigned v) { return __uint_as_float(v << 16); }
__device__ __forceinline__ float hi16(unsigned v) { return __uint_as_float(v & 0xffff0000u); }

constexpr int PREP_WGS = 214;
constexpr int PREP_UNIT_LDS = 22784;

__device__ __forceinline__ int hi_value(int idx) {
  int v;
  if (idx < 16) v = 128 * (idx >> 3) + (idx & 7);
  else if (idx < 24) v = 256 + 128 * ((idx - 16) >> 2) + 2 * ((idx - 16) & 3);
  else if (idx < 32) v = 512 + 128 * ((idx - 24) >> 1) + 4 * ((idx - 24) & 1);
  else if (idx <= 40) v = 1024 + 128 * (idx - 32);
  else v = 0;
  return v;
}

__device__ __forceinline__ u32x4 tr_frag(unsigned addr0, unsigned addr1) {
  s16x4 a = __builtin_amdgcn_ds_read_tr16_b64_v4i16((LDS_AS s16x4*)(size_t)addr0);
  s16x4 c = __builtin_amdgcn_ds_read_tr16_b64_v4i16((LDS_AS s16x4*)(size_t)addr1);
  u32x2 a2 = __builtin_bit_cast(u32x2, a), c2 = __builtin_bit_cast(u32x2, c);
  return (u32x4){a2[0], a2[1], c2[0], c2[1]};
}
__device__ __forceinline__ void split2(float a, float b, unsigned& hi, unsigned& lo) {
  const unsigned ua = __float_as_uint(a), ub = __float_as_uint(b);
  hi = __builtin_amdgcn_perm(ub, ua, 0x07060302u);
  lo = pk2(a - __uint_as_float(ua & 0xffff0000u), b - __uint_as_float(ub & 0xffff0000u));
}

__global__ __launch_bounds__(1024) void k_prep(const u16* __restrict__ x, const u16* __restrict__ logA,
                                               const u16* __restrict__ Aim, const u16* __restrict__ Cre,
                                               const u16* __restrict__ Cim, const u16* __restrict__ logdt,
                                               const u16* __restrict__ wq, const u16* __restrict__ wk,
                                               const u16* __restrict__ wv, const u16* __restrict__ wo,
                                               char* __restrict__ ws) {
  __shared__ __attribute__((aligned(16))) char smem_all[4 * PREP_UNIT_LDS];
  const int ug = __builtin_amdgcn_readfirstlane(threadIdx.x >> 8);
  char* smem = smem_all + ug * PREP_UNIT_LDS;
  const int t = threadIdx.x & 255, lane = t & 63, w = __builtin_amdgcn_readfirstlane(t >> 6);
  const unsigned sbase = (unsigned)(size_t)smem;
  const int g = lane >> 4, li = lane & 15, q4 = li >> 2, p4 = li & 3;
  const int b = blockIdx.x;
  if (ug < 3) {
    const int d = min(b * 3 + ug, D_ - 1);
    STAMP(ws, 0, 0);
    float* prm = (float*)smem;
    float* part = prm + 512;
    float* tab = part + 4 * 768;
    float* red = tab + 2056;
    if (t < 128) {
      const float dt = rbf(expf(bf2f(logdt[d])));
      float ar = -rbf(expf(bf2f(logA[d * NS_ + t])));
      float alpha = ar * dt, beta = bf2f(Aim[d * NS_ + t]) * dt;
      f32x4 pv = {alpha * LOG2E, beta * INV2PI, 2.0f * (bf2f(Cre[d * NS_ + t]) * dt), 2.0f * (bf2f(Cim[d * NS_ + t]) * dt)};
      *(f32x4*)(prm + t * 4) = pv;
      float bnd = fabsf(pv[2]) + fabsf(pv[3]);
#pragma unroll
      for (int o = 32; o > 0; o >>= 1) bnd += __shfl_xor(bnd, o);
      if (lane == 0) red[w] = bnd;
    }
    __syncthreads();
    STAMP(ws, 0, 1);
    const float lo = 8.0f * li;
    float hv[3] = {(float)hi_value(li), (float)hi_value(li + 16), (float)hi_value(li + 32)};
    f32x4 acc[3] = {{0, 0, 0, 0}, {0, 0, 0, 0}, {0, 0, 0, 0}};
#pragma unroll 1
    for (int ks = 0; ks < 2; ks++) {
      u32x4 bh, bl, ah[3], al[3];
#pragma unroll
      for (int m = 0; m < 4; m++) {
        const int n = 32 * w + 16 * ks + 4 * g + m;
        const f32x4 p = *(const f32x4*)(prm + n * 4);
        {
          float e = __builtin_amdgcn_exp2f(p[0] * lo);
          float ph = __builtin_amdgcn_fractf(p[1] * lo);
          unsigned h_, l_;
          split2(e * __builtin_amdgcn_cosf(ph), e * __builtin_amdgcn_sinf(ph), h_, l_);
          bh[m] = h_; bl[m] = l_;
        }
#pragma unroll
        for (int tt = 0; tt < 3; tt++) {
          float e2 = __builtin_amdgcn_exp2f(p[0] * hv[tt]);
          float ph2 = __builtin_amdgcn_fractf(p[1] * hv[tt]);
          float er = e2 * __builtin_amdgcn_cosf(ph2), ei = e2 * __builtin_amdgcn_sinf(ph2);
          float Ar = p[2] * er - p[3] * ei;
          float nAi = -(p[2] * ei + p[3] * er);
          unsigned h_, l_;
          split2(Ar, nAi, h_, l_);
          ah[tt][m] = h_; al[tt][m] = l_;
        }
      }
#pragma unroll
      for (int tt = 0; tt < 3; tt++) {
        acc[tt] = __builtin_amdgcn_mfma_f32_16x16x32_bf16(__builtin_bit_cast(bf16x8, al[tt]), __builtin_bit_cast(bf16x8, bh), acc[tt], 0, 0, 0);
        acc[tt] = __builtin_amdgcn_mfma_f32_16x16x32_bf16(__builtin_bit_cast(bf16x8, ah[tt]), __builtin_bit_cast(bf16x8, bl), acc[tt], 0, 0, 0);
        acc[tt] = __builtin_amdgcn_mfma_f32_16x16x32_bf16(__builtin_bit_cast(bf16x8, ah[tt]), __builtin_bit_cast(bf16x8, bh), acc[tt], 0, 0, 0);
      }
    }
    STAMP(ws, 0, 2);
#pragma unroll
    for (int tt = 0; tt < 3; tt++)
#pragma unroll
      for (int r = 0; r < 4; r++) part[w * 768 + (16 * tt + g * 4 + r) * 16 + li] = acc[tt][r];
    __syncthreads();
#pragma unroll
    for (int k = 0; k < 3; k++) {
      const int i = t + 256 * k;
      const int hi = i >> 4, lo_i = i & 15;
      if (hi < 40 || (hi == 40 && lo_i == 0))
        tab[hi_value(hi) + 8 * lo_i] = (part[i] + part[768 + i]) + (part[1536 + i] + part[2304 + i]);
    }
    __syncthreads();
    STAMP(ws, 0, 3);
    const float bnd = red[0] + red[1];
    int ex = (int)((__float_as_uint(bnd) >> 23) & 0xff) - 126;
    if (!(bnd > 1e-30f && bnd < 1e30f)) ex = 15;
    const float sc = __uint_as_float((unsigned)(127 + 15 - ex) << 23);
    f16x8 o;
#pragma unroll
    for (int e = 0; e < 8; e++) o[e] = (_Float16)(tab[(int)rbf((float)(8 * t + e))] * sc);
    f16x8* dst = (f16x8*)(ws + OFF_KF + (size_t)d * KFROW * 2);
    dst[31 + t] = o;
    if (t < 32) {
      f16x8 z = {0, 0, 0, 0, 0, 0, 0, 0};
      dst[t < 31 ? t : 287] = z;
    }
    if (t == 0) ((float*)(ws + OFF_KINV))[d] = __uint_as_float((unsigned)(127 + ex - 15 - 9) << 23);
    STAMP(ws, 0, 4);
  } else if (b < 128) {
    __syncthreads();
    const int l0 = b * 16, row = t >> 4, part = t & 15;
    const u32x4* src = (const u32x4*)(x + (size_t)(l0 + row) * D_ + part * 40);
    u32x4 v[5];
#pragma unroll
    for (int i = 0; i < 5; i++) v[i] = src[i];
    if (t < 128) {
      const int c = b * 128 + t;
      const int piece = (c >> 6) & 3, ln = c & 63;
      const unsigned cv = (piece >= 2 && (ln & 31) == 20) ? 0x3F803F80u : 0u;
      u32x4 o = {cv, cv, cv, cv};
      ((u32x4*)(ws + OFF_KV))[c] = o;
    }
    float s = 0.f;
#pragma unroll
    for (int i = 0; i < 5; i++)
#pragma unroll
      for (int j = 0; j < 4; j++) s += lo16(v[i][j]) + hi16(v[i][j]);
    s += __shfl_xor(s, 1); s += __shfl_xor(s, 2);
    const float mean = s * (1.0f / 160.0f);
    float m2 = 0.f;
#pragma unroll
    for (int i = 0; i < 5; i++)
#pragma unroll
      for (int j = 0; j < 4; j++) {
        float a = lo16(v[i][j]) - mean, c = hi16(v[i][j]) - mean;
        m2 += a * a + c * c;
      }
    m2 += __shfl_xor(m2, 1); m2 += __shfl_xor(m2, 2);
    if ((part & 3) == 0) {
      f32x2 o = {mean, m2};
      ((f32x2*)(ws + OFF_ST0))[(part >> 2) * L_ + l0 + row] = o;
    }
#pragma unroll
    for (int i = 0; i < 5; i++) *(u32x4*)(smem + row * 1296 + part * 80 + i * 16) = v[i];
    __syncthreads();
    u16* xt = (u16*)(ws + OFF_XT0);
#pragma unroll
    for (int i = 0; i < 5; i++) {
      const int blk = (w * 5 + i) * 4 + g;
      const int ct = blk >> 1, rh = blk & 1;
      const unsigned ad = sbase + (rh * 8 + q4) * 1296 + ct * 32 + p4 * 8;
      const u32x4 f = tr_frag(ad, ad + 4 * 1296);
      *(u32x4*)(xt + (size_t)(ct * 16 + li) * L_ + l0 + rh * 8) = f;
    }
    __syncthreads();
  } else if (b < 208) {
    __syncthreads();
    const int u = b - 128;
    const int hf = u & 1, uu = u >> 1;
    const bool isq = uu < 20;
    const int ks = isq ? uu : uu - 20;
    const u16* W = isq ? wq : wo;
    u32x4* dst = (u32x4*)(ws + (isq ? OFF_WQKV : OFF_WO));
    u32x4 v[5];
#pragma unroll
    for (int i = 0; i < 5; i++) {
      const int c = t + 256 * i;
      const int r = c / 40, cc = c - r * 40;
      v[i] = *(const u32x4*)(W + (size_t)(ks * 32 + r) * 640 + hf * 320 + cc * 8);
    }
    const bool dokv = isq && hf == 0;
    u16 e[8];
    const int nkv = (40 + w) * 16 + li;
    if (dokv) {
#pragma unroll
      for (int j = 0; j < 8; j++) {
        const int k = ks * 32 + 8 * g + j;
        e[j] = (nkv < 660) ? wk[k * 20 + (nkv - 640)] : (nkv < 680) ? wv[k * 20 + (nkv - 660)] : (u16)0;
      }
    }
#pragma unroll
    for (int i = 0; i < 5; i++) {
      const int c = t + 256 * i;
      const int r = c / 40, cc = c - r * 40;
      *(u32x4*)(smem + r * 656 + cc * 16) = v[i];
    }
    __syncthreads();
#pragma unroll
    for (int i = 0; i < 5; i++) {
      const int nt = w * 5 + i;
      const unsigned ad = sbase + (8 * g + q4) * 656 + nt * 32 + p4 * 8;
      const u32x4 f = tr_frag(ad, ad + 4 * 656);
      dst[((size_t)(hf * 20 + nt) * 20 + ks) * 64 + lane] = f;
    }
    if (dokv) {
      u32x4 o;
#pragma unroll
      for (int j = 0; j < 4; j++) o[j] = (unsigned)e[2 * j] | ((unsigned)e[2 * j + 1] << 16);
      dst[((size_t)(40 + w) * 20 + ks) * 64 + lane] = o;
    }
    __syncthreads();
  } else {
    __syncthreads();
    const int u = b - 208;
    const int c8 = t & 15, rg = t >> 4;
    float sacc[8] = {0, 0, 0, 0, 0, 0, 0, 0};
    if (u < 5) {
      const u32x4* src = (const u32x4*)(wq + (size_t)(rg * 40) * 640 + u * 128 + c8 * 8);
#pragma unroll
      for (int hh = 0; hh < 2; hh++) {
        u32x4 v[20];
#pragma unroll
        for (int i = 0; i < 20; i++) v[i] = src[(hh * 20 + i) * 80];
#pragma unroll
        for (int i = 0; i < 20; i++)
#pragma unroll
          for (int j = 0; j < 4; j++) { sacc[2 * j] += lo16(v[i][j]); sacc[2 * j + 1] += hi16(v[i][j]); }
      }
    } else if (c8 < 10) {
      const u16* src = ((c8 < 5) ? wk + c8 * 4 : wv + (c8 - 5) * 4) + (size_t)(rg * 40) * 20;
      u32x2 v[40];
#pragma unroll
      for (int i = 0; i < 40; i++) v[i] = *(const u32x2*)(src + i * 20);
#pragma unroll
      for (int i = 0; i < 40; i++) { sacc[0] += lo16(v[i][0]); sacc[1] += hi16(v[i][0]); sacc[2] += lo16(v[i][1]); sacc[3] += hi16(v[i][1]); }
    }
#pragma unroll
    for (int j = 0; j < 8; j++) { sacc[j] += __shfl_xor(sacc[j], 16); sacc[j] += __shfl_xor(sacc[j], 32); }
    float* red = (float*)smem;
    if (lane < 16) {
#pragma unroll
      for (int j = 0; j < 8; j++) red[w * 128 + lane * 8 + j] = sacc[j];
    }
    __syncthreads();
    if (t < 128) {
      const float v = (red[t] + red[128 + t]) + (red[256 + t] + red[384 + t]);
      if (u < 5) ((float*)(ws + OFF_CS))[u * 128 + t] = v;
      else if (t < 64) {
        const int src_i = (t >> 2) * 8 + (t & 3);
        const float v2 = (red[src_i] + red[128 + src_i]) + (red[256 + src_i] + red[384 + src_i]);
        ((float*)(ws + OFF_CS))[640 + t] = (t < 40) ? v2 : 0.f;
      }
    }
    __syncthreads();
  }
}

constexpr int CV_COPY = 321;
constexpr int CV_KOFF = 2576;
constexpr int CV_CH = CV_KOFF + 288;
constexpr int CV_ZERO = 3 * CV_CH;
constexpr int CV_STAGE = CV_ZERO + 48;
constexpr int CV_STAT = CV_STAGE + 771;
constexpr int CV_TOTAL = CV_STAT + 512;

template <bool GEN>
__global__ __launch_bounds__(768) void k_conv(const u16* __restrict__ xT, const f32x2* __restrict__ st,
                                              char* __restrict__ ws, const u16* __restrict__ dskip,
                                              u16* __restrict__ outT, const u16* __restrict__ logA,
                                              const u16* __restrict__ Aim, const u16* __restrict__ Cre,
                                              const u16* __restrict__ Cim, const u16* __restrict__ logdt) {
  __shared__ __attribute__((aligned(16))) u32x4 sm[CV_TOTAL];
  const int tg = threadIdx.x;
  const int cgp = __builtin_amdgcn_readfirstlane(tg >> 8), t = tg & 255, lane = t & 63;
  const int w = __builtin_amdgcn_readfirstlane(t >> 6);
  const int dreal = blockIdx.x * 3 + cgp;
  const bool valid = dreal < D_;
  const int d = valid ? dreal : D_ - 1;
  const int ci = lane & 31, h = lane >> 5;
  u32x4* cb = sm + cgp * CV_CH;
  STAMP(ws, 1, 0);
  u16 pa_ = 0, pb_ = 0, pc_ = 0, pd_ = 0, pe_ = 0;
  if (GEN) {
    pe_ = logdt[d];
    if (t < 128) { pa_ = logA[d * NS_ + t]; pb_ = Aim[d * NS_ + t]; pc_ = Cre[d * NS_ + t]; pd_ = Cim[d * NS_ + t]; }
  }
  f32x2 sp[3][4];
#pragma unroll
  for (int i = 0; i < 3; i++) {
    const int row = min(tg + 768 * i, L_ - 1);
#pragma unroll
    for (int p = 0; p < 4; p++) sp[i][p] = st[p * L_ + row];
  }
  const u32x4 xv = *(const u32x4*)(xT + (size_t)d * L_ + 8 * t);
  u32x4* kg = (u32x4*)(ws + OFF_KF + (size_t)d * KFROW * 2);
  u32x4 k0v = {0, 0, 0, 0}, k1v = {0, 0, 0, 0};
  float kinv = 0.f;
  if (!GEN) {
    k0v = kg[t];
    if (t < 32) k1v = kg[256 + t];
    kinv = ((const float*)(ws + OFF_KINV))[d];
  }
  const int lo0 = 8 * ci + 4 * h + 256 * w;
  const u32x2 xo0 = *(const u32x2*)(xT + (size_t)d * L_ + lo0);
  const u32x2 xo1 = *(const u32x2*)(xT + (size_t)d * L_ + lo0 + 1024);
  const float dsk = bf2f(dskip[d]);
  if (tg < 48) { u32x4 z = {0, 0, 0, 0}; sm[CV_ZERO + tg] = z; }
  float* prm = (float*)cb;
  float* part = prm + 512;
  float* tab = part + 4 * 768;
  float* red = tab + 2056;
  if (GEN) {
    if (t < 128) {
      const float dt = rbf(expf(bf2f(pe_)));
      float ar = -rbf(expf(bf2f(pa_)));
      float alpha = ar * dt, beta = bf2f(pb_) * dt;
      f32x4 pv = {alpha * LOG2E, beta * INV2PI, 2.0f * (bf2f(pc_) * dt), 2.0f * (bf2f(pd_) * dt)};
      *(f32x4*)(prm + t * 4) = pv;
      float bnd = fabsf(pv[2]) + fabsf(pv[3]);
#pragma unroll
      for (int o = 32; o > 0; o >>= 1) bnd += __shfl_xor(bnd, o);
      if (lane == 0) red[w] = bnd;
    }
    __syncthreads();
    const int g = lane >> 4, idx = lane & 15;
    const float lo = 8.0f * idx;
    float hv[3] = {(float)hi_value(idx), (float)hi_value(idx + 16), (float)hi_value(idx + 32)};
    f32x4 acc[3] = {{0, 0, 0, 0}, {0, 0, 0, 0}, {0, 0, 0, 0}};
#pragma unroll 2
    for (int gi = 0; gi < 8; gi++) {
      const int n = 4 * (8 * w + gi) + g;
      const f32x4 p = *(const f32x4*)(prm + n * 4);
      float e = __builtin_amdgcn_exp2f(p[0] * lo);
      float ph = __builtin_amdgcn_fractf(p[1] * lo);
      const float blr = e * __builtin_amdgcn_cosf(ph), bli = e * __builtin_amdgcn_sinf(ph);
#pragma unroll
      for (int tt = 0; tt < 3; tt++) {
        float e2 = __builtin_amdgcn_exp2f(p[0] * hv[tt]);
        float ph2 = __builtin_amdgcn_fractf(p[1] * hv[tt]);
        float er = e2 * __builtin_amdgcn_cosf(ph2), ei = e2 * __builtin_amdgcn_sinf(ph2);
        float Ar = p[2] * er - p[3] * ei;
        float Ai = p[2] * ei + p[3] * er;
        acc[tt] = __builtin_amdgcn_mfma_f32_16x16x4f32(Ar, blr, acc[tt], 0, 0, 0);
        acc[tt] = __builtin_amdgcn_mfma_f32_16x16x4f32(-Ai, bli, acc[tt], 0, 0, 0);
      }
    }
#pragma unroll
    for (int tt = 0; tt < 3; tt++)
#pragma unroll
      for (int r = 0; r < 4; r++) part[w * 768 + (16 * tt + g * 4 + r) * 16 + idx] = acc[tt][r];
  }
#pragma unroll
  for (int i = 0; i < 3; i++) {
    const int row = tg + 768 * i;
    float m0 = sp[i][0][0], m1 = sp[i][1][0], m2 = sp[i][2][0], m3 = sp[i][3][0];
    float mean = 0.25f * ((m0 + m1) + (m2 + m3));
    float a0 = m0 - mean, a1 = m1 - mean, a2 = m2 - mean, a3 = m3 - mean;
    float M2 = ((sp[i][0][1] + sp[i][1][1]) + (sp[i][2][1] + sp[i][3][1])) +
               160.0f * ((a0 * a0 + a1 * a1) + (a2 * a2 + a3 * a3));
    float vb = rbf(M2 * (1.0f / 640.0f));
    float sd = __builtin_amdgcn_sqrtf(rbf(vb + 1e-5f));
    if (row < L_) ((unsigned*)(sm + CV_STAT))[row] = pk2(mean, sd);
  }
  if (!GEN) {
    cb[CV_KOFF + t] = k0v;
    if (t < 32) cb[CV_KOFF + 256 + t] = k1v;
  }
  __syncthreads();
  if (GEN) {
#pragma unroll
    for (int k = 0; k < 3; k++) {
      const int i = t + 256 * k;
      const int hi = i >> 4, lo_i = i & 15;
      if (hi < 40 || (hi == 40 && lo_i == 0))
        tab[hi_value(hi) + 8 * lo_i] = (part[i] + part[768 + i]) + (part[1536 + i] + part[2304 + i]);
    }
    __syncthreads();
    const float bnd = red[0] + red[1];
    int ex = (int)((__float_as_uint(bnd) >> 23) & 0xff) - 126;
    if (!(bnd > 1e-30f && bnd < 1e30f)) ex = 15;
    const float sc = __uint_as_float((unsigned)(127 + 15 - ex) << 23);
    kinv = __uint_as_float((unsigned)(127 + ex - 15 - 9) << 23);
    f16x8 o;
#pragma unroll
    for (int e = 0; e < 8; e++) o[e] = (_Float16)(tab[(int)rbf((float)(8 * t + e))] * sc);
    const u32x4 ov = __builtin_bit_cast(u32x4, o);
    cb[CV_KOFF + 31 + t] = ov;
    if (valid) kg[31 + t] = ov;
    if (t < 32) {
      u32x4 z = {0, 0, 0, 0};
      cb[CV_KOFF + (t < 31 ? t : 287)] = z;
      if (valid) kg[t < 31 ? t : 287] = z;
    }
    if (t == 0 && valid) ((float*)(ws + OFF_KINV))[d] = kinv;
  }
  unsigned cw[4];
  {
    const u32x4 s0 = sm[CV_STAT + 2 * t], s1 = sm[CV_STAT + 2 * t + 1];
#pragma unroll
    for (int i = 0; i < 4; i++) {
      const unsigned sa = i < 2 ? s0[2 * i] : s1[2 * i - 4], sb = i < 2 ? s0[2 * i + 1] : s1[2 * i - 3];
      float u0 = rbf(rbf(lo16(xv[i]) - lo16(sa)) * __builtin_amdgcn_rcpf(hi16(sa)));
      float u1 = rbf(rbf(hi16(xv[i]) - lo16(sb)) * __builtin_amdgcn_rcpf(hi16(sb)));
      _Float16 f0 = (_Float16)(u0 * 512.0f), f1 = (_Float16)(u1 * 512.0f);
      cw[i] = (unsigned)__builtin_bit_cast(u16, f0) | ((unsigned)__builtin_bit_cast(u16, f1) << 16);
    }
    u32x4 c4 = {cw[0], cw[1], cw[2], cw[3]};
    sm[CV_STAGE + cgp * 257 + 1 + t] = c4;
    if (t == 0) { u32x4 z = {0, 0, 0, 0}; sm[CV_STAGE + cgp * 257] = z; }
  }
  STAMP(ws, 1, 1);
  __syncthreads();
  {
    const u32x4 pv = sm[CV_STAGE + cgp * 257 + t];
    unsigned Z[8], Y[7];
#pragma unroll
    for (int m = 0; m < 4; m++) {
      Z[m] = __builtin_amdgcn_alignbit(cw[3 - m], cw[3 - m], 16);
      Z[4 + m] = __builtin_amdgcn_alignbit(pv[3 - m], pv[3 - m], 16);
    }
#pragma unroll
    for (int m = 0; m < 7; m++) Y[m] = __builtin_amdgcn_alignbit(Z[m + 1], Z[m], 16);
    const int phys = (t >> 5) * 40 + (t & 31);
#pragma unroll
    for (int r = 0; r < 8; r++) {
      const int o = 7 - r;
      u32x4 v;
      if ((o & 1) == 0) { v[0] = Z[o / 2]; v[1] = Z[o / 2 + 1]; v[2] = Z[o / 2 + 2]; v[3] = Z[o / 2 + 3]; }
      else { v[0] = Y[o / 2]; v[1] = Y[o / 2 + 1]; v[2] = Y[o / 2 + 2]; v[3] = Y[o / 2 + 3]; }
      cb[r * CV_COPY + phys] = v;
    }
  }
  __syncthreads();
  STAMP(ws, 1, 2);
  f32x16 T0 = {0}, T1 = {0};
  {
    const int r = ci & 7, al = ci >> 3;
    const int blo = al - w, bhi = al + 4 - w;
    const int ulo = (blo >= 0) ? (cgp * CV_CH + r * CV_COPY + 40 * blo + 1 - h) : (CV_ZERO + r + 8 * (blo & 1) + 1 - h);
    const int uhi = cgp * CV_CH + r * CV_COPY + 40 * bhi + 1 - h;
    const int ka = cgp * CV_CH + CV_KOFF + ci + h + 32 * w;
#pragma unroll 4
    for (int s = 0; s < 16; s++) {
      const u32x4 a_lo = sm[ulo + 30 - 2 * s];
      const u32x4 a_hi = sm[uhi + 30 - 2 * s];
      const u32x4 b_a = sm[ka + 2 * s];
      const u32x4 b_b = sm[ka + 2 * s + 128];
      T0 = __builtin_amdgcn_mfma_f32_32x32x16_f16(__builtin_bit_cast(f16x8, a_lo), __builtin_bit_cast(f16x8, b_a), T0, 0, 0, 0);
      T1 = __builtin_amdgcn_mfma_f32_32x32x16_f16(__builtin_bit_cast(f16x8, a_hi), __builtin_bit_cast(f16x8, b_a), T1, 0, 0, 0);
      T1 = __builtin_amdgcn_mfma_f32_32x32x16_f16(__builtin_bit_cast(f16x8, a_lo), __builtin_bit_cast(f16x8, b_b), T1, 0, 0, 0);
    }
  }
  STAMP(ws, 1, 3);
  __syncthreads();
  f32x4* redc = (f32x4*)cb;
#pragma unroll
  for (int g = 0; g < 4; g++) {
    f32x4 a = {T0[4 * g], T0[4 * g + 1], T0[4 * g + 2], T0[4 * g + 3]};
    f32x4 c = {T1[4 * g], T1[4 * g + 1], T1[4 * g + 2], T1[4 * g + 3]};
    redc[((w * 2 + 0) * 4 + g) * 64 + lane] = a;
    redc[((w * 2 + 1) * 4 + g) * 64 + lane] = c;
  }
  __syncthreads();
#pragma unroll
  for (int T = 0; T < 2; T++) {
    f32x4 y = (redc[((0 * 2 + T) * 4 + w) * 64 + lane] + redc[((1 * 2 + T) * 4 + w) * 64 + lane]) +
              (redc[((2 * 2 + T) * 4 + w) * 64 + lane] + redc[((3 * 2 + T) * 4 + w) * 64 + lane]);
    const int l = lo0 + 1024 * T;
    const int g = l >> 3;
    const u32x2 ur = *((const u32x2*)(cb + 7 * CV_COPY + (g >> 5) * 40 + (g & 31)) + (1 - h));
    float uu[4];
    uu[3] = (float)__builtin_bit_cast(_Float16, (u16)(ur[0] & 0xffffu)) * (1.0f / 512.0f);
    uu[2] = (float)__builtin_bit_cast(_Float16, (u16)(ur[0] >> 16)) * (1.0f / 512.0f);
    uu[1] = (float)__builtin_bit_cast(_Float16, (u16)(ur[1] & 0xffffu)) * (1.0f / 512.0f);
    uu[0] = (float)__builtin_bit_cast(_Float16, (u16)(ur[1] >> 16)) * (1.0f / 512.0f);
    const u32x2 xo = T ? xo1 : xo0;
    float xx[4] = {lo16(xo[0]), hi16(xo[0]), lo16(xo[1]), hi16(xo[1])};
    float r[4];
#pragma unroll
    for (int e = 0; e < 4; e++) {
      float z = rbf(rbf(y[e] * kinv) + rbf(uu[e] * dsk));
      r[e] = xx[e] + z;
    }
    u32x2 o = {pk2(r[0], r[1]), pk2(r[2], r[3])};
    if (valid) *(u32x2*)(outT + (size_t)d * L_ + l) = o;
  }
  STAMP(ws, 1, 4);
}

template <int MODE>
__global__ __launch_bounds__(MODE == 0 ? 704 : 640) void k_gemm(const u16* __restrict__ XT, const u32x4* __restrict__ Wp,
                                                                const u16* __restrict__ resT, u16* __restrict__ out,
                                                                char* __restrict__ ws, f32x2* __restrict__ stout) {
  constexpr int NT = (MODE == 0) ? 11 : 10;
  constexpr int NTH = NT * 64;
  __shared__ __attribute__((aligned(16))) char smem[40960 + 32 * 164 * 4];
  const int t = threadIdx.x, lane = t & 63, w = __builtin_amdgcn_readfirstlane(t >> 6);
  const int b = blockIdx.x;
  const int cg = (b >> 3) & 3, rb = (b & 7) + 8 * (b >> 5);
  const int l0 = rb * 32;
  const int g = lane >> 4, li = lane & 15;
  const int ntile = cg * NT + w;
  STAMP(ws, 2 + MODE, 0);
  u32x4 xv[4];
#pragma unroll
  for (int i = 0; i < 4; i++) {
    const int c = t + NTH * i;
    if (c < 2560) xv[i] = *(const u32x4*)(XT + (size_t)(c >> 2) * L_ + l0 + (c & 3) * 8);
  }
  const u32x4* wp0 = Wp + (size_t)ntile * 20 * 64 + lane;
  u32x4 wf[20];
#pragma unroll
  for (int p = 0; p < 3; p++) wf[p] = wp0[p * 64];
  __builtin_amdgcn_sched_barrier(0);
#pragma unroll
  for (int i = 0; i < 4; i++) {
    const int c = t + NTH * i;
    const int row = c >> 2;
    if (c < 2560) *(u32x4*)(smem + row * 64 + (((c & 3) ^ (2 * ((row >> 3) & 1))) * 16)) = xv[i];
  }
  __syncthreads();
  STAMP(ws, 2 + MODE, 1);
  f32x4 acc[2] = {{0, 0, 0, 0}, {0, 0, 0, 0}};
  const int q4 = li >> 2, p4 = li & 3;
  const unsigned sbase = (unsigned)(size_t)smem;
  unsigned xa[2];
#pragma unroll
  for (int lt = 0; lt < 2; lt++)
    xa[lt] = sbase + (8 * g + q4) * 64 + (((2 * lt + (p4 >> 1)) ^ (2 * (g & 1))) * 16) + 8 * (p4 & 1);
  f32x2* stp = (f32x2*)(smem + 40960);
  if (MODE == 0) {
    float ssum[2] = {0.f, 0.f}, ssq[2] = {0.f, 0.f};
    const bf16x2 ones = __builtin_bit_cast(bf16x2, 0x3F803F80u);
#pragma unroll
    for (int kk = 0; kk < 2; kk++) {
      const int ks = w + NT * kk;
      if (ks < 20) {
#pragma unroll
        for (int lt = 0; lt < 2; lt++) {
          const u32x4 xf = tr_frag(xa[lt] + ks * 2048, xa[lt] + ks * 2048 + 256);
#pragma unroll
          for (int e = 0; e < 4; e++) {
            const unsigned ux = xf[e];
            bf16x2 xx = __builtin_bit_cast(bf16x2, ux);
            ssum[lt] = __builtin_amdgcn_fdot2_f32_bf16(xx, ones, ssum[lt], false);
            ssq[lt] = __builtin_amdgcn_fdot2_f32_bf16(xx, xx, ssq[lt], false);
          }
        }
      }
    }
#pragma unroll
    for (int lt = 0; lt < 2; lt++) {
      ssum[lt] += __shfl_xor(ssum[lt], 16); ssum[lt] += __shfl_xor(ssum[lt], 32);
      ssq[lt] += __shfl_xor(ssq[lt], 16); ssq[lt] += __shfl_xor(ssq[lt], 32);
      if (g == 0) { f32x2 o = {ssum[lt], ssq[lt]}; stp[w * 32 + 16 * lt + li] = o; }
    }
  }
  __builtin_amdgcn_sched_barrier(0);
#pragma unroll
  for (int p = 3; p < 20; p++) wf[p] = wp0[p * 64];
  f32x4 c4;
  u32x2 xr[2];
  u16 xs[2][4];
  if (MODE == 0) c4 = *(const f32x4*)((const float*)(ws + OFF_CS) + ntile * 16 + 4 * g);
  if (MODE == 1) {
#pragma unroll
    for (int lt = 0; lt < 2; lt++) xr[lt] = *(const u32x2*)(resT + (size_t)(ntile * 16 + li) * L_ + l0 + 16 * lt + 4 * g);
  }
  if (MODE == 2) {
#pragma unroll
    for (int lt = 0; lt < 2; lt++)
#pragma unroll
      for (int e = 0; e < 4; e++) xs[lt][e] = resT[(size_t)(ntile * 16 + 4 * g + e) * L_ + l0 + 16 * lt + li];
  }
#pragma unroll
  for (int ks = 0; ks < 20; ks++) {
    u32x4 xf[2];
#pragma unroll
    for (int lt = 0; lt < 2; lt++) xf[lt] = tr_frag(xa[lt] + ks * 2048, xa[lt] + ks * 2048 + 256);
    const bf16x8 wv = __builtin_bit_cast(bf16x8, wf[ks]);
#pragma unroll
    for (int lt = 0; lt < 2; lt++) {
      if (MODE == 1) acc[lt] = __builtin_amdgcn_mfma_f32_16x16x32_bf16(__builtin_bit_cast(bf16x8, xf[lt]), wv, acc[lt], 0, 0, 0);
      else acc[lt] = __builtin_amdgcn_mfma_f32_16x16x32_bf16(wv, __builtin_bit_cast(bf16x8, xf[lt]), acc[lt], 0, 0, 0);
    }
  }
  STAMP(ws, 2 + MODE, 2);
  if (MODE == 0) {
    __syncthreads();
    f32x2 svv[2];
#pragma unroll
    for (int lt = 0; lt < 2; lt++) {
      f32x2 a = {0.f, 0.f};
#pragma unroll
      for (int i = 0; i < 11; i++) a += stp[i * 32 + 16 * lt + li];
      float mean = a[0] * (1.0f / 640.0f);
      float var = fmaxf(a[1] * (1.0f / 640.0f) - mean * mean, 0.f);
      float sd = rbf(__builtin_amdgcn_sqrtf(rbf(rbf(var) + 1e-5f)));
      svv[lt] = (f32x2){rbf(mean), __builtin_amdgcn_rcpf(sd)};
    }
    u16* qo = (u16*)(ws + OFF_Q);
    char* kvimg = ws + OFF_KV;
    const int n0 = ntile * 16 + 4 * g;
#pragma unroll
    for (int lt = 0; lt < 2; lt++) {
      const f32x2 sv = svv[lt];
      const int l = l0 + 16 * lt + li;
      float v[4];
#pragma unroll
      for (int r = 0; r < 4; r++) v[r] = (acc[lt][r] - sv[0] * c4[r]) * sv[1];
      if (n0 < 640) {
        u32x2 o = {pk2(v[0] * QSCALE, v[1] * QSCALE), pk2(v[2] * QSCALE, v[3] * QSCALE)};
        *(u32x2*)(qo + (size_t)l * D_ + n0) = o;
      } else if (n0 < 660) {
        const int dh = n0 - 640;
        u32x2 o = {pk2(v[0], v[1]), pk2(v[2], v[3])};
        const int tile = l >> 5, step = dh >> 4, ln = (l & 31) + 32 * ((dh & 15) >> 3);
        *(u32x2*)(kvimg + tile * 4096 + step * 1024 + ln * 16 + (dh & 7) * 2) = o;
      } else if (n0 < 680) {
        const int dv = n0 - 660;
        const int tile = l >> 5, kk = l & 15, step = (l & 31) >> 4;
        const int hh = (kk & 7) >> 2, jj = (kk >> 3) * 4 + (kk & 3);
#pragma unroll
        for (int r = 0; r < 4; r++)
          *(u16*)(kvimg + tile * 4096 + 2048 + step * 1024 + (dv + r + 32 * hh) * 16 + jj * 2) = f2bf(v[r]);
      }
    }
  } else if (MODE == 1) {
    float* stg = (float*)(smem + 40960);
    const int nl = w * 16 + li;
    const int n = cg * 160 + nl;
#pragma unroll
    for (int lt = 0; lt < 2; lt++) {
      const int ll = 16 * lt + 4 * g;
      float r[4];
      r[0] = rbf(lo16(xr[lt][0]) + rbf(acc[lt][0]));
      r[1] = rbf(hi16(xr[lt][0]) + rbf(acc[lt][1]));
      r[2] = rbf(lo16(xr[lt][1]) + rbf(acc[lt][2]));
      r[3] = rbf(hi16(xr[lt][1]) + rbf(acc[lt][3]));
      u32x2 o = {pk2(r[0], r[1]), pk2(r[2], r[3])};
      *(u32x2*)(out + (size_t)n * L_ + l0 + ll) = o;
#pragma unroll
      for (int e = 0; e < 4; e++) stg[(ll + e) * 164 + nl] = r[e];
    }
    __syncthreads();
    if (t < 256) {
      const int row = t >> 3, part = t & 7;
      float v[20];
      float s = 0.f;
#pragma unroll
      for (int i = 0; i < 5; i++) {
        f32x4 a = *(const f32x4*)(stg + row * 164 + part * 20 + 4 * i);
#pragma unroll
        for (int e = 0; e < 4; e++) { v[4 * i + e] = a[e]; s += a[e]; }
      }
      s += __shfl_xor(s, 1); s += __shfl_xor(s, 2); s += __shfl_xor(s, 4);
      const float mean = s * (1.0f / 160.0f);
      float m2 = 0.f;
#pragma unroll
      for (int i = 0; i < 20; i++) { float a = v[i] - mean; m2 += a * a; }
      m2 += __shfl_xor(m2, 1); m2 += __shfl_xor(m2, 2); m2 += __shfl_xor(m2, 4);
      if (part == 0) { f32x2 o = {mean, m2}; stout[cg * L_ + l0 + row] = o; }
    }
  } else {
    const int n0 = ntile * 16 + 4 * g;
#pragma unroll
    for (int lt = 0; lt < 2; lt++) {
      const int l = l0 + 16 * lt + li;
      float r[4];
#pragma unroll
      for (int e = 0; e < 4; e++) r[e] = bf2f(xs[lt][e]) + rbf(acc[lt][e]);
      u32x2 o = {pk2(r[0], r[1]), pk2(r[2], r[3])};
      *(u32x2*)(out + (size_t)l * D_ + n0) = o;
    }
  }
  STAMP(ws, 2 + MODE, 3);
}

__device__ __forceinline__ void pack_p(const f32x16& s, bf16x8& p0, bf16x8& p1) {
  u32x4 a, c;
#pragma unroll
  for (int i = 0; i < 4; i++) {
    a[i] = pk2(__builtin_amdgcn_exp2f(s[2 * i]), __builtin_amdgcn_exp2f(s[2 * i + 1]));
    c[i] = pk2(__builtin_amdgcn_exp2f(s[8 + 2 * i]), __builtin_amdgcn_exp2f(s[9 + 2 * i]));
  }
  p0 = __builtin_bit_cast(bf16x8, a);
  p1 = __builtin_bit_cast(bf16x8, c);
}
__device__ __forceinline__ float max16(const f32x16& s) {
  float m = fmaxf(s[0], s[1]);
#pragma unroll
  for (int i = 2; i < 16; i++) m = fmaxf(m, s[i]);
  return m;
}

__global__ __launch_bounds__(1024) void k_attn(const char* __restrict__ ws_q, const u32x4* __restrict__ kv,
                                               u16* __restrict__ oT) {
  __shared__ __attribute__((aligned(16))) u32x4 sm[2][2048];
  const int t = threadIdx.x, lane = t & 63, w = __builtin_amdgcn_readfirstlane(t >> 6);
  const int b = blockIdx.x;
  const int kh = w >> 3;
  const int pb = b >> 2, head = (b & 3) * 8 + (w & 7);
  const int ci = lane & 31, h = lane >> 5;
  const int l = pb * 32 + ci;
  STAMP(ws_q - OFF_Q, 5, 0);
  u32x4 stg[2];
  stg[0] = kv[t];
  stg[1] = kv[8192 + t];
  bf16x8 q0, q1;
  {
    const char* qp = ws_q + (size_t)l * (D_ * 2) + head * 40;
    u32x2 a = *(const u32x2*)(qp + 16 * h), c = *(const u32x2*)(qp + 16 * h + 8);
    u32x2 e = *(const u32x2*)(qp + 32);
    u32x4 f0 = {a[0], a[1], c[0], c[1]};
    u32x4 f1 = {h ? 0u : e[0], h ? 0u : e[1], 0u, 0u};
    q0 = __builtin_bit_cast(bf16x8, f0);
    q1 = __builtin_bit_cast(bf16x8, f1);
  }
  sm[0][t] = stg[0];
  sm[0][1024 + t] = stg[1];
  __syncthreads();
  STAMP(ws_q - OFF_Q, 5, 1);
  STAMPC(ws_q - OFF_Q, 6, 0);
  f32x16 negm;
  {
    f32x16 z = {0};
    f32x16 s = __builtin_amdgcn_mfma_f32_32x32x16_bf16(__builtin_bit_cast(bf16x8, sm[0][lane]), q0, z, 0, 0, 0);
    s = __builtin_amdgcn_mfma_f32_32x32x16_bf16(__builtin_bit_cast(bf16x8, sm[0][64 + lane]), q1, s, 0, 0, 0);
    float m = max16(s);
    m = fmaxf(m, __shfl_xor(m, 32));
#pragma unroll
    for (int i = 0; i < 16; i++) negm[i] = -m;
  }
  f32x16 o = {0};
  for (int c = 0; c < 8; c++) {
    const u32x4* buf = sm[c & 1] + kh * 1024 + lane;
    if (c + 1 < 8) {
      stg[0] = kv[(c + 1) * 1024 + t];
      stg[1] = kv[8192 + (c + 1) * 1024 + t];
    }
    {
#define SB() __builtin_amdgcn_sched_barrier(0)
      u32x4 k0n = buf[0], k1n = buf[64];
      u32x4 v0 = buf[128], v1 = buf[192];
      SB();
      f32x16 s = __builtin_amdgcn_mfma_f32_32x32x16_bf16(__builtin_bit_cast(bf16x8, k0n), q0, negm, 0, 0, 0);
      s = __builtin_amdgcn_mfma_f32_32x32x16_bf16(__builtin_bit_cast(bf16x8, k1n), q1, s, 0, 0, 0);
      k0n = buf[256]; k1n = buf[256 + 64];
      SB();
#pragma unroll
      for (int tl = 0; tl < 4; tl++) {
        f32x16 sn;
        u32x4 pa, pb;
        if (tl < 3) sn = __builtin_amdgcn_mfma_f32_32x32x16_bf16(__builtin_bit_cast(bf16x8, k0n), q0, negm, 0, 0, 0);
        SB();
        pa[0] = pk2(__builtin_amdgcn_exp2f(s[0]), __builtin_amdgcn_exp2f(s[1]));
        pa[1] = pk2(__builtin_amdgcn_exp2f(s[2]), __builtin_amdgcn_exp2f(s[3]));
        SB();
        if (tl < 3) sn = __builtin_amdgcn_mfma_f32_32x32x16_bf16(__builtin_bit_cast(bf16x8, k1n), q1, sn, 0, 0, 0);
        if (tl < 2) { k0n = buf[(tl + 2) * 256]; k1n = buf[(tl + 2) * 256 + 64]; }
        SB();
        pa[2] = pk2(__builtin_amdgcn_exp2f(s[4]), __builtin_amdgcn_exp2f(s[5]));
        pa[3] = pk2(__builtin_amdgcn_exp2f(s[6]), __builtin_amdgcn_exp2f(s[7]));
        pb[0] = pk2(__builtin_amdgcn_exp2f(s[8]), __builtin_amdgcn_exp2f(s[9]));
        SB();
        o = __builtin_amdgcn_mfma_f32_32x32x16_bf16(__builtin_bit_cast(bf16x8, v0), __builtin_bit_cast(bf16x8, pa), o, 0, 0, 0);
        if (tl < 3) v0 = buf[(tl + 1) * 256 + 128];
        SB();
        pb[1] = pk2(__builtin_amdgcn_exp2f(s[10]), __builtin_amdgcn_exp2f(s[11]));
        pb[2] = pk2(__builtin_amdgcn_exp2f(s[12]), __builtin_amdgcn_exp2f(s[13]));
        pb[3] = pk2(__builtin_amdgcn_exp2f(s[14]), __builtin_amdgcn_exp2f(s[15]));
        SB();
        o = __builtin_amdgcn_mfma_f32_32x32x16_bf16(__builtin_bit_cast(bf16x8, v1), __builtin_bit_cast(bf16x8, pb), o, 0, 0, 0);
        if (tl < 3) { v1 = buf[(tl + 1) * 256 + 192]; s = sn; }
        SB();
      }
    }
    if (c + 1 < 8) {
      sm[(c + 1) & 1][t] = stg[0];
      sm[(c + 1) & 1][1024 + t] = stg[1];
    }
    __syncthreads();
  }
  STAMP(ws_q - OFF_Q, 5, 2);
  STAMPC(ws_q - OFF_Q, 6, 1);
  f32x4* mg = (f32x4*)&sm[0][0];
  if (kh == 1) {
#pragma unroll
    for (int i = 0; i < 3; i++) {
      f32x4 a = {o[4 * i], o[4 * i + 1], o[4 * i + 2], o[4 * i + 3]};
      mg[((w & 7) * 3 + i) * 64 + lane] = a;
    }
  }
  __syncthreads();
  if (kh == 1) return;
#pragma unroll
  for (int i = 0; i < 3; i++) {
    f32x4 a = mg[(w * 3 + i) * 64 + lane];
#pragma unroll
    for (int e = 0; e < 4; e++) o[4 * i + e] += a[e];
  }
  float lsum = __shfl(o[8], ci + 32);
  bool bad = !(lsum <= 1.8446744e19f);
#pragma unroll
  for (int i = 0; i < 12; i++) bad = bad || !(fabsf(o[i]) <= 3.0e38f);
  if (__any(bad)) {
    float m = -INFINITY;
#pragma unroll
    for (int i = 0; i < 16; i++) o[i] = 0.f;
    for (int tl = 0; tl < 64; tl++) {
      f32x16 z = {0};
      f32x16 s = __builtin_amdgcn_mfma_f32_32x32x16_bf16(__builtin_bit_cast(bf16x8, kv[tl * 256 + lane]), q0, z, 0, 0, 0);
      s = __builtin_amdgcn_mfma_f32_32x32x16_bf16(__builtin_bit_cast(bf16x8, kv[tl * 256 + 64 + lane]), q1, s, 0, 0, 0);
      float tm = max16(s);
      tm = fmaxf(tm, __shfl_xor(tm, 32));
      const float mn = fmaxf(m, tm);
      const float al = __builtin_amdgcn_exp2f(m - mn);
      m = mn;
#pragma unroll
      for (int i = 0; i < 16; i++) { o[i] *= al; s[i] -= mn; }
      bf16x8 p0, p1;
      pack_p(s, p0, p1);
      o = __builtin_amdgcn_mfma_f32_32x32x16_bf16(__builtin_bit_cast(bf16x8, kv[tl * 256 + 128 + lane]), p0, o, 0, 0, 0);
      o = __builtin_amdgcn_mfma_f32_32x32x16_bf16(__builtin_bit_cast(bf16x8, kv[tl * 256 + 192 + lane]), p1, o, 0, 0, 0);
    }
    lsum = __shfl(o[8], ci + 32);
  }
  const float inv = 1.0f / lsum;
  u16* op = oT + (size_t)(head * 20 + 4 * h) * L_ + l;
#pragma unroll
  for (int i = 0; i < 12; i++) {
    const int dd = (i & 3) + 8 * (i >> 2);
    if (i < 8 || h == 0) op[(size_t)dd * L_] = f2bf(o[i] * inv);
  }
  STAMP(ws_q - OFF_Q, 5, 3);
}

extern "C" void kernel_launch(void* const* d_in, const int* in_sizes, int n_in, void* d_out, int out_size,
                              void* d_ws, size_t ws_size, hipStream_t stream) {
  const u16* x = (const u16*)d_in[0];
  const u16* logA = (const u16*)d_in[1];
  const u16* Aim = (const u16*)d_in[2];
  const u16* Cre = (const u16*)d_in[3];
  const u16* Cim = (const u16*)d_in[4];
  const u16* logdt = (const u16*)d_in[5];
  const u16* dskip = (const u16*)d_in[6];
  const u16* wq = (const u16*)d_in[8];
  const u16* wk = (const u16*)d_in[9];
  const u16* wv = (const u16*)d_in[10];
  const u16* wo = (const u16*)d_in[11];
  char* ws = (char*)d_ws;
  u16* xt0 = (u16*)(ws + OFF_XT0);
  u16* x1t = (u16*)(ws + OFF_X1T);
  u16* x2t = (u16*)(ws + OFF_X2T);
  u16* x3t = (u16*)(ws + OFF_X3T);
  u16* ot = (u16*)(ws + OFF_OT);
  const u32x4* wqkv = (const u32x4*)(ws + OFF_WQKV);
  const u32x4* wop = (const u32x4*)(ws + OFF_WO);
  const u32x4* kv = (const u32x4*)(ws + OFF_KV);
  f32x2* st0 = (f32x2*)(ws + OFF_ST0);
  f32x2* st1 = (f32x2*)(ws + OFF_ST1);

  k_prep<<<PREP_WGS, 1024, 0, stream>>>(x, logA, Aim, Cre, Cim, logdt, wq, wk, wv, wo, ws);
  k_conv<false><<<214, 768, 0, stream>>>(xt0, st0, ws, dskip, x1t, logA, Aim, Cre, Cim, logdt);
  k_gemm<0><<<256, 704, 0, stream>>>(x1t, wqkv, nullptr, nullptr, ws, nullptr);
  k_attn<<<256, 1024, 0, stream>>>(ws + OFF_Q, kv, ot);
  k_gemm<1><<<256, 640, 0, stream>>>(ot, wop, x1t, x2t, ws, st1);
  k_conv<false><<<214, 768, 0, stream>>>(x2t, st1, ws, dskip, x3t, logA, Aim, Cre, Cim, logdt);
  k_gemm<0><<<256, 704, 0, stream>>>(x3t, wqkv, nullptr, nullptr, ws, nullptr);
  k_attn<<<256, 1024, 0, stream>>>(ws + OFF_Q, kv, ot);
  k_gemm<2><<<256, 640, 0, stream>>>(ot, wop, x3t, (u16*)d_out, ws, nullptr);
}
```

```cpp
#include <hip/hip_runtime.h>
#include <hip/hip_bf16.h>
#include <stdint.h>

typedef unsigned short u16;
typedef __attribute__((ext_vector_type(8))) __bf16 bf16x8;
typedef __attribute__((ext_vector_type(8))) _Float16 f16x8;
typedef __attribute__((ext_vector_type(16))) float f32x16;
typedef __attribute__((ext_vector_type(4))) float f32x4;
typedef __attribute__((ext_vector_type(4))) unsigned u32x4;
typedef __attribute__((ext_vector_type(2))) unsigned u32x2;
typedef __attribute__((ext_vector_type(4))) short s16x4;
typedef __attribute__((ext_vector_type(2))) float f32x2;

#define LDS_AS __attribute__((address_space(3)))
#define STAMP(wsp, kid, i)
#define STAMPC(wsp, kid, i)

constexpr int L_ = 2048, D_ = 640, NS_ = 128;
constexpr int KFROW = 2304;
constexpr float LOG2E = 1.4426950408889634f;
constexpr float INV2PI = 0.15915494309189535f;
constexpr float QSCALE = 0.22360679774997896f * 1.4426950408889634f;

constexpr size_t SZ_ACT = (size_t)D_ * L_ * 2;
constexpr size_t OFF_XT0 = 0;
constexpr size_t OFF_X1T = OFF_XT0 + SZ_ACT;
constexpr size_t OFF_X2T = OFF_X1T + SZ_ACT;
constexpr size_t OFF_X3T = OFF_X2T + SZ_ACT;
constexpr size_t OFF_Q = OFF_X3T + SZ_ACT;
constexpr size_t OFF_OT = OFF_Q + SZ_ACT;
constexpr size_t OFF_KF = OFF_OT + SZ_ACT;
constexpr size_t OFF_KINV = OFF_KF + (size_t)D_ * KFROW * 2;
constexpr size_t OFF_WQKV = OFF_KINV + 4096;
constexpr size_t OFF_WO = OFF_WQKV + (size_t)44 * 20 * 1024;
constexpr size_t OFF_CS = OFF_WO + (size_t)40 * 20 * 1024;
constexpr size_t OFF_KV = OFF_CS + 4096;
constexpr size_t OFF_ST0 = OFF_KV + 262144;
constexpr size_t OFF_ST1 = OFF_ST0 + 65536;

__device__ __forceinline__ float bf2f(u16 v) { return __uint_as_float(((unsigned)v) << 16); }
__device__ __forceinline__ u16 f2bf(float f) { __bf16 b = (__bf16)f; return __builtin_bit_cast(u16, b); }
__device__ __forceinline__ float rbf(float f) { return bf2f(f2bf(f)); }
typedef __attribute__((ext_vector_type(2))) __bf16 bf16x2;
__device__ __forceinline__ unsigned pk2(float a, float b) {
  f32x2 v = {a, b};
  return __builtin_bit_cast(unsigned, __builtin_convertvector(v, bf16x2));
}
__device__ __forceinline__ float lo16(unsigned v) { return __uint_as_float(v << 16); }
__device__ __forceinline__ float hi16(unsigned v) { return __uint_as_float(v & 0xffff0000u); }

constexpr int PREP_WGS = 256;
constexpr int PREP_UNIT_LDS = 23808;

__device__ __forceinline__ int hi_value(int idx) {
  int v;
  if (idx < 16) v = 128 * (idx >> 3) + (idx & 7);
  else if (idx < 24) v = 256 + 128 * ((idx - 16) >> 2) + 2 * ((idx - 16) & 3);
  else if (idx < 32) v = 512 + 128 * ((idx - 24) >> 1) + 4 * ((idx - 24) & 1);
  else if (idx <= 40) v = 1024 + 128 * (idx - 32);
  else v = 0;
  return v;
}

__device__ __forceinline__ u32x4 tr_frag(unsigned addr0, unsigned addr1) {
  s16x4 a = __builtin_amdgcn_ds_read_tr16_b64_v4i16((LDS_AS s16x4*)(size_t)addr0);
  s16x4 c = __builtin_amdgcn_ds_read_tr16_b64_v4i16((LDS_AS s16x4*)(size_t)addr1);
  u32x2 a2 = __builtin_bit_cast(u32x2, a), c2 = __builtin_bit_cast(u32x2, c);
  return (u32x4){a2[0], a2[1], c2[0], c2[1]};
}
__device__ __forceinline__ void split2(float a, float b, unsigned& hi, unsigned& lo) {
  const unsigned ua = __float_as_uint(a), ub = __float_as_uint(b);
  hi = __builtin_amdgcn_perm(ub, ua, 0x07060302u);
  lo = pk2(a - __uint_as_float(ua & 0xffff0000u), b - __uint_as_float(ub & 0xffff0000u));
}

__global__ __launch_bounds__(1024) void k_prep(const u16* __restrict__ x, const u16* __restrict__ logA,
                                               const u16* __restrict__ Aim, const u16* __restrict__ Cre,
                                               const u16* __restrict__ Cim, const u16* __restrict__ logdt,
                                               const u16* __restrict__ wq, const u16* __restrict__ wk,
                                               const u16* __restrict__ wv, const u16* __restrict__ wo,
                                               char* __restrict__ ws) {
  __shared__ __attribute__((aligned(16))) char smem_all[4 * PREP_UNIT_LDS];
  const int ug = __builtin_amdgcn_readfirstlane(threadIdx.x >> 8);
  char* smem = smem_all + ug * PREP_UNIT_LDS;
  const int t = threadIdx.x & 255, lane = t & 63, w = __builtin_amdgcn_readfirstlane(t >> 6);
  const unsigned sbase = (unsigned)(size_t)smem;
  const int g = lane >> 4, li = lane & 15, q4 = li >> 2, p4 = li & 3;
  const int b = blockIdx.x;
  if (ug < 3 && b >= 214) {
    __syncthreads(); __syncthreads(); __syncthreads();
  } else if (ug < 3) {
    const int d = min(b * 3 + ug, D_ - 1);
    STAMP(ws, 0, 0);
    float* prm = (float*)smem;
    float* part = prm + 512;
    float* tab = part + 4 * 768;
    float* red = tab + 2312;
    if (t < 128) {
      const float dt = rbf(expf(bf2f(logdt[d])));
      float ar = -rbf(expf(bf2f(logA[d * NS_ + t])));
      float alpha = ar * dt, beta = bf2f(Aim[d * NS_ + t]) * dt;
      const float cr = 2.0f * (bf2f(Cre[d * NS_ + t]) * dt), ci = 2.0f * (bf2f(Cim[d * NS_ + t]) * dt);
      float bnd = hypotf(cr, ci);
      f32x4 pv = {alpha * LOG2E, beta * INV2PI, log2f(bnd), atan2f(ci, cr) * INV2PI};
      *(f32x4*)(prm + t * 4) = pv;
#pragma unroll
      for (int o = 32; o > 0; o >>= 1) bnd += __shfl_xor(bnd, o);
      if (lane == 0) red[w] = bnd;
    }
    __syncthreads();
    STAMP(ws, 0, 1);
    const float lo = 8.0f * li;
    float hv[3] = {(float)hi_value(li), (float)hi_value(li + 16), (float)hi_value(li + 32)};
    f32x4 acc[3] = {{0, 0, 0, 0}, {0, 0, 0, 0}, {0, 0, 0, 0}};
#pragma unroll 1
    for (int ks = 0; ks < 2; ks++) {
      u32x4 bh, bl, ah[3], al[3];
#pragma unroll
      for (int m = 0; m < 4; m++) {
        const int n = 32 * w + 16 * ks + 4 * g + m;
        const f32x4 p = *(const f32x4*)(prm + n * 4);
        {
          float e = __builtin_amdgcn_exp2f(p[0] * lo);
          float ph = __builtin_amdgcn_fractf(p[1] * lo);
          unsigned h_, l_;
          split2(e * __builtin_amdgcn_cosf(ph), e * __builtin_amdgcn_sinf(ph), h_, l_);
          bh[m] = h_; bl[m] = l_;
        }
#pragma unroll
        for (int tt = 0; tt < 3; tt++) {
          float e2 = __builtin_amdgcn_exp2f(fmaf(p[0], hv[tt], p[2]));
          float ph2 = __builtin_amdgcn_fractf(fmaf(p[1], hv[tt], p[3]));
          unsigned h_, l_;
          split2(e2 * __builtin_amdgcn_cosf(ph2), -e2 * __builtin_amdgcn_sinf(ph2), h_, l_);
          ah[tt][m] = h_; al[tt][m] = l_;
        }
      }
#pragma unroll
      for (int tt = 0; tt < 3; tt++) {
        acc[tt] = __builtin_amdgcn_mfma_f32_16x16x32_bf16(__builtin_bit_cast(bf16x8, al[tt]), __builtin_bit_cast(bf16x8, bh), acc[tt], 0, 0, 0);
        acc[tt] = __builtin_amdgcn_mfma_f32_16x16x32_bf16(__builtin_bit_cast(bf16x8, ah[tt]), __builtin_bit_cast(bf16x8, bl), acc[tt], 0, 0, 0);
        acc[tt] = __builtin_amdgcn_mfma_f32_16x16x32_bf16(__builtin_bit_cast(bf16x8, ah[tt]), __builtin_bit_cast(bf16x8, bh), acc[tt], 0, 0, 0);
      }
    }
    STAMP(ws, 0, 2);
#pragma unroll
    for (int tt = 0; tt < 3; tt++)
#pragma unroll
      for (int r = 0; r < 4; r++) part[w * 768 + (16 * tt + g * 4 + r) * 16 + li] = acc[tt][r];
    __syncthreads();
#pragma unroll
    for (int k = 0; k < 3; k++) {
      const int i = t + 256 * k;
      const int hi = i >> 4, lo_i = i & 15;
      if (hi < 40 || (hi == 40 && lo_i == 0))
      {
        const int pp = hi_value(hi) + 8 * lo_i;
        tab[pp + (pp >> 3)] = (part[i] + part[768 + i]) + (part[1536 + i] + part[2304 + i]);
      }
    }
    __syncthreads();
    STAMP(ws, 0, 3);
    const float bnd = red[0] + red[1];
    int ex = (int)((__float_as_uint(bnd) >> 23) & 0xff) - 126;
    if (!(bnd > 1e-30f && bnd < 1e30f)) ex = 15;
    const float sc = __uint_as_float((unsigned)(127 + 15 - ex) << 23);
    f16x8 o;
#pragma unroll
    for (int e = 0; e < 8; e++) {
      const int pp = (int)rbf((float)(8 * t + e));
      o[e] = (_Float16)(tab[pp + (pp >> 3)] * sc);
    }
    f16x8* dst = (f16x8*)(ws + OFF_KF + (size_t)d * KFROW * 2);
    dst[31 + t] = o;
    if (t < 32) {
      f16x8 z = {0, 0, 0, 0, 0, 0, 0, 0};
      dst[t < 31 ? t : 287] = z;
    }
    if (t == 0) ((float*)(ws + OFF_KINV))[d] = __uint_as_float((unsigned)(127 + ex - 15 - 9) << 23);
    STAMP(ws, 0, 4);
  } else if (b < 128) {
    __syncthreads();
    const int l0 = b * 16, row = t >> 4, part = t & 15;
    const u32x4* src = (const u32x4*)(x + (size_t)(l0 + row) * D_ + part * 40);
    u32x4 v[5];
#pragma unroll
    for (int i = 0; i < 5; i++) v[i] = src[i];
    if (t < 128) {
      const int c = b * 128 + t;
      const int piece = (c >> 6) & 3, ln = c & 63;
      const unsigned cv = (piece >= 2 && (ln & 31) == 20) ? 0x3F803F80u : 0u;
      u32x4 o = {cv, cv, cv, cv};
      ((u32x4*)(ws + OFF_KV))[c] = o;
    }
    float s = 0.f;
#pragma unroll
    for (int i = 0; i < 5; i++)
#pragma unroll
      for (int j = 0; j < 4; j++) s += lo16(v[i][j]) + hi16(v[i][j]);
    s += __shfl_xor(s, 1); s += __shfl_xor(s, 2);
    const float mean = s * (1.0f / 160.0f);
    float m2 = 0.f;
#pragma unroll
    for (int i = 0; i < 5; i++)
#pragma unroll
      for (int j = 0; j < 4; j++) {
        float a = lo16(v[i][j]) - mean, c = hi16(v[i][j]) - mean;
        m2 += a * a + c * c;
      }
    m2 += __shfl_xor(m2, 1); m2 += __shfl_xor(m2, 2);
    if ((part & 3) == 0) {
      f32x2 o = {mean, m2};
      ((f32x2*)(ws + OFF_ST0))[(part >> 2) * L_ + l0 + row] = o;
    }
#pragma unroll
    for (int i = 0; i < 5; i++) *(u32x4*)(smem + row * 1296 + part * 80 + i * 16) = v[i];
    __syncthreads();
    u16* xt = (u16*)(ws + OFF_XT0);
#pragma unroll
    for (int i = 0; i < 5; i++) {
      const int blk = (w * 5 + i) * 4 + g;
      const int ct = blk >> 1, rh = blk & 1;
      const unsigned ad = sbase + (rh * 8 + q4) * 1296 + ct * 32 + p4 * 8;
      const u32x4 f = tr_frag(ad, ad + 4 * 1296);
      *(u32x4*)(xt + (size_t)(ct * 16 + li) * L_ + l0 + rh * 8) = f;
    }
    __syncthreads();
  } else if (b < 208) {
    __syncthreads();
    const int u = b - 128;
    const int hf = u & 1, uu = u >> 1;
    const bool isq = uu < 20;
    const int ks = isq ? uu : uu - 20;
    const u16* W = isq ? wq : wo;
    u32x4* dst = (u32x4*)(ws + (isq ? OFF_WQKV : OFF_WO));
    u32x4 v[5];
#pragma unroll
    for (int i = 0; i < 5; i++) {
      const int c = t + 256 * i;
      const int r = c / 40, cc = c - r * 40;
      v[i] = *(const u32x4*)(W + (size_t)(ks * 32 + r) * 640 + hf * 320 + cc * 8);
    }
    const bool dokv = isq && hf == 0;
    u16 e[8];
    const int nkv = (40 + w) * 16 + li;
    if (dokv) {
#pragma unroll
      for (int j = 0; j < 8; j++) {
        const int k = ks * 32 + 8 * g + j;
        e[j] = (nkv < 660) ? wk[k * 20 + (nkv - 640)] : (nkv < 680) ? wv[k * 20 + (nkv - 660)] : (u16)0;
      }
    }
#pragma unroll
    for (int i = 0; i < 5; i++) {
      const int c = t + 256 * i;
      const int r = c / 40, cc = c - r * 40;
      *(u32x4*)(smem + r * 656 + cc * 16) = v[i];
    }
    __syncthreads();
#pragma unroll
    for (int i = 0; i < 5; i++) {
      const int nt = w * 5 + i;
      const unsigned ad = sbase + (8 * g + q4) * 656 + nt * 32 + p4 * 8;
      const u32x4 f = tr_frag(ad, ad + 4 * 656);
      dst[((size_t)(hf * 20 + nt) * 20 + ks) * 64 + lane] = f;
    }
    if (dokv) {
      u32x4 o;
#pragma unroll
      for (int j = 0; j < 4; j++) o[j] = (unsigned)e[2 * j] | ((unsigned)e[2 * j + 1] << 16);
      dst[((size_t)(40 + w) * 20 + ks) * 64 + lane] = o;
    }
    __syncthreads();
  } else if (b < 214) {
    __syncthreads(); __syncthreads(); __syncthreads();
  } else {
    __syncthreads();
    const int u = min(b - 214, 40);
    float sacc[8] = {0, 0, 0, 0, 0, 0, 0, 0};
    float* red = (float*)smem;
    if (u < 40) {
      const int c2 = t & 1, rg = t >> 1;
      const u32x4* src = (const u32x4*)(wq + (size_t)(rg * 5) * 640 + u * 16 + c2 * 8);
      u32x4 v[5];
#pragma unroll
      for (int i = 0; i < 5; i++) v[i] = src[i * 80];
#pragma unroll
      for (int i = 0; i < 5; i++)
#pragma unroll
        for (int j = 0; j < 4; j++) { sacc[2 * j] += lo16(v[i][j]); sacc[2 * j + 1] += hi16(v[i][j]); }
#pragma unroll
      for (int j = 0; j < 8; j++)
#pragma unroll
        for (int o = 2; o < 64; o <<= 1) sacc[j] += __shfl_xor(sacc[j], o);
      if (lane < 2) {
#pragma unroll
        for (int j = 0; j < 8; j++) red[w * 16 + lane * 8 + j] = sacc[j];
      }
    } else {
      const int c10 = t % 10, rg = t / 10;
      if (rg < 25) {
        const u16* src = ((c10 < 5) ? wk + c10 * 4 : wv + (c10 - 5) * 4);
#pragma unroll
        for (int i = 0; i < 26; i++) {
          const int row = rg + 25 * i;
          if (row < 640) {
            const u32x2 v = *(const u32x2*)(src + (size_t)row * 20);
            sacc[0] += lo16(v[0]); sacc[1] += hi16(v[0]); sacc[2] += lo16(v[1]); sacc[3] += hi16(v[1]);
          }
        }
#pragma unroll
        for (int j = 0; j < 4; j++) red[rg * 40 + c10 * 4 + j] = sacc[j];
      }
    }
    __syncthreads();
    if (u < 40) {
      if (t < 16) ((float*)(ws + OFF_CS))[u * 16 + t] = (red[t] + red[16 + t]) + (red[32 + t] + red[48 + t]);
    } else if (t < 64) {
      float v = 0.f;
      if (t < 40) {
#pragma unroll
        for (int i = 0; i < 25; i++) v += red[i * 40 + t];
      }
      ((float*)(ws + OFF_CS))[640 + t] = v;
    }
    __syncthreads();
  }
}

constexpr int CV_COPY = 321;
constexpr int CV_UOFF = 2576;
constexpr int CV_CH = CV_UOFF + 288;
constexpr int CV_ZERO = 3 * CV_CH;
constexpr int CV_STAT = CV_ZERO + 48;
constexpr int CV_TOTAL = CV_STAT + 512;

__global__ __launch_bounds__(768) void k_conv(const u16* __restrict__ xT, const f32x2* __restrict__ st,
                                              const char* __restrict__ ws, const u16* __restrict__ dskip,
                                              u16* __restrict__ outT) {
  __shared__ __attribute__((aligned(16))) u32x4 sm[CV_TOTAL];
  const int tg = threadIdx.x;
  const int cgp = __builtin_amdgcn_readfirstlane(tg >> 8), t = tg & 255, lane = t & 63;
  const int w = __builtin_amdgcn_readfirstlane(t >> 6);
  const int dreal = blockIdx.x * 3 + cgp;
  const bool valid = dreal < D_;
  const int d = valid ? dreal : D_ - 1;
  const int ci = lane & 31, h = lane >> 5;
  u32x4* cb = sm + cgp * CV_CH;
  STAMP(ws, 1, 0);
  const u32x4* kg = (const u32x4*)(ws + OFF_KF + (size_t)d * KFROW * 2);
  const u32x4 kc = kg[31 + t], kp = kg[30 + t];
  f32x2 sp[3][4];
#pragma unroll
  for (int i = 0; i < 3; i++) {
    const int row = min(tg + 768 * i, L_ - 1);
#pragma unroll
    for (int p = 0; p < 4; p++) sp[i][p] = st[p * L_ + row];
  }
  const u32x4 xv = *(const u32x4*)(xT + (size_t)d * L_ + 8 * t);
  const float kinv = ((const float*)(ws + OFF_KINV))[d];
  const int lo0 = 8 * ci + 4 * h + 256 * w;
  const u32x2 xo0 = *(const u32x2*)(xT + (size_t)d * L_ + lo0);
  const u32x2 xo1 = *(const u32x2*)(xT + (size_t)d * L_ + lo0 + 1024);
  const float dsk = bf2f(dskip[d]);
  {
    u32x4 z = {0, 0, 0, 0};
    if (tg < 48) sm[CV_ZERO + tg] = z;
    if (t < 32) cb[CV_UOFF + (t < 31 ? t : 287)] = z;
  }
  {
    unsigned Z[8], Y[7];
#pragma unroll
    for (int m = 0; m < 4; m++) {
      Z[m] = __builtin_amdgcn_alignbit(kc[3 - m], kc[3 - m], 16);
      Z[4 + m] = __builtin_amdgcn_alignbit(kp[3 - m], kp[3 - m], 16);
    }
#pragma unroll
    for (int m = 0; m < 7; m++) Y[m] = __builtin_amdgcn_alignbit(Z[m + 1], Z[m], 16);
    const int phys = (t >> 5) * 40 + (t & 31);
#pragma unroll
    for (int r = 0; r < 8; r++) {
      const int o = 7 - r;
      u32x4 v;
      if ((o & 1) == 0) { v[0] = Z[o / 2]; v[1] = Z[o / 2 + 1]; v[2] = Z[o / 2 + 2]; v[3] = Z[o / 2 + 3]; }
      else { v[0] = Y[o / 2]; v[1] = Y[o / 2 + 1]; v[2] = Y[o / 2 + 2]; v[3] = Y[o / 2 + 3]; }
      cb[r * CV_COPY + phys] = v;
    }
  }
#pragma unroll
  for (int i = 0; i < 3; i++) {
    const int row = tg + 768 * i;
    float m0 = sp[i][0][0], m1 = sp[i][1][0], m2 = sp[i][2][0], m3 = sp[i][3][0];
    float mean = 0.25f * ((m0 + m1) + (m2 + m3));
    float a0 = m0 - mean, a1 = m1 - mean, a2 = m2 - mean, a3 = m3 - mean;
    float M2 = ((sp[i][0][1] + sp[i][1][1]) + (sp[i][2][1] + sp[i][3][1])) +
               160.0f * ((a0 * a0 + a1 * a1) + (a2 * a2 + a3 * a3));
    float vb = rbf(M2 * (1.0f / 640.0f));
    float sd = __builtin_amdgcn_sqrtf(rbf(vb + 1e-5f));
    if (row < L_) ((unsigned*)(sm + CV_STAT))[row] = pk2(mean, sd);
  }
  __syncthreads();
  STAMP(ws, 1, 1);
  {
    const u32x4 s0 = sm[CV_STAT + 2 * t], s1 = sm[CV_STAT + 2 * t + 1];
    u32x4 c4;
#pragma unroll
    for (int i = 0; i < 4; i++) {
      const unsigned sa = i < 2 ? s0[2 * i] : s1[2 * i - 4], sb = i < 2 ? s0[2 * i + 1] : s1[2 * i - 3];
      float u0 = rbf(rbf(lo16(xv[i]) - lo16(sa)) * __builtin_amdgcn_rcpf(hi16(sa)));
      float u1 = rbf(rbf(hi16(xv[i]) - lo16(sb)) * __builtin_amdgcn_rcpf(hi16(sb)));
      _Float16 f0 = (_Float16)(u0 * 512.0f), f1 = (_Float16)(u1 * 512.0f);
      c4[i] = (unsigned)__builtin_bit_cast(u16, f0) | ((unsigned)__builtin_bit_cast(u16, f1) << 16);
    }
    cb[CV_UOFF + 31 + t] = c4;
  }
  __syncthreads();
  STAMP(ws, 1, 2);
  STAMPC(ws, 10, 0);
  f32x16 T0 = {0}, T1 = {0};
  {
    const int r = ci & 7, al = ci >> 3;
    const int blo = al - w, bhi = al + 4 - w;
    const int alo = (blo >= 0) ? (cgp * CV_CH + r * CV_COPY + 40 * blo + 1 - h) : (CV_ZERO + r + 8 * (blo & 1) + 1 - h);
    const int ahi = cgp * CV_CH + r * CV_COPY + 40 * bhi + 1 - h;
    const int ua = cgp * CV_CH + CV_UOFF + ci + h + 32 * w;
    u32x4 a_lo = sm[alo + 30], a_hi = sm[ahi + 30], b_a = sm[ua], b_b = sm[ua + 128];
#pragma unroll
    for (int s = 0; s < 16; s++) {
      u32x4 n_lo, n_hi, n_a, n_b;
      if (s < 15) {
        n_lo = sm[alo + 28 - 2 * s]; n_hi = sm[ahi + 28 - 2 * s];
        n_a = sm[ua + 2 * s + 2]; n_b = sm[ua + 2 * s + 130];
      }
      __builtin_amdgcn_sched_barrier(0);
      T0 = __builtin_amdgcn_mfma_f32_32x32x16_f16(__builtin_bit_cast(f16x8, a_lo), __builtin_bit_cast(f16x8, b_a), T0, 0, 0, 0);
      T1 = __builtin_amdgcn_mfma_f32_32x32x16_f16(__builtin_bit_cast(f16x8, a_hi), __builtin_bit_cast(f16x8, b_a), T1, 0, 0, 0);
      T1 = __builtin_amdgcn_mfma_f32_32x32x16_f16(__builtin_bit_cast(f16x8, a_lo), __builtin_bit_cast(f16x8, b_b), T1, 0, 0, 0);
      __builtin_amdgcn_sched_barrier(0);
      if (s < 15) { a_lo = n_lo; a_hi = n_hi; b_a = n_a; b_b = n_b; }
    }
  }
  STAMP(ws, 1, 3);
  __syncthreads();
  STAMPC(ws, 10, 1);
  STAMP(ws, 1, 5);
  f32x4* redc = (f32x4*)cb;
#pragma unroll
  for (int g = 0; g < 4; g++) {
    f32x4 a = {T0[4 * g], T0[4 * g + 1], T0[4 * g + 2], T0[4 * g + 3]};
    f32x4 c = {T1[4 * g], T1[4 * g + 1], T1[4 * g + 2], T1[4 * g + 3]};
    redc[((w * 2 + 0) * 4 + g) * 64 + lane] = a;
    redc[((w * 2 + 1) * 4 + g) * 64 + lane] = c;
  }
  __syncthreads();
#pragma unroll
  for (int T = 0; T < 2; T++) {
    f32x4 y = (redc[((0 * 2 + T) * 4 + w) * 64 + lane] + redc[((1 * 2 + T) * 4 + w) * 64 + lane]) +
              (redc[((2 * 2 + T) * 4 + w) * 64 + lane] + redc[((3 * 2 + T) * 4 + w) * 64 + lane]);
    const int l = lo0 + 1024 * T;
    const u32x2 ur = *((const u32x2*)(cb + CV_UOFF + 31 + (l >> 3)) + h);
    float uu[4];
    uu[0] = (float)__builtin_bit_cast(_Float16, (u16)(ur[0] & 0xffffu)) * (1.0f / 512.0f);
    uu[1] = (float)__builtin_bit_cast(_Float16, (u16)(ur[0] >> 16)) * (1.0f / 512.0f);
    uu[2] = (float)__builtin_bit_cast(_Float16, (u16)(ur[1] & 0xffffu)) * (1.0f / 512.0f);
    uu[3] = (float)__builtin_bit_cast(_Float16, (u16)(ur[1] >> 16)) * (1.0f / 512.0f);
    const u32x2 xo = T ? xo1 : xo0;
    float xx[4] = {lo16(xo[0]), hi16(xo[0]), lo16(xo[1]), hi16(xo[1])};
    float r[4];
#pragma unroll
    for (int e = 0; e < 4; e++) {
      float z = rbf(rbf(y[e] * kinv) + rbf(uu[e] * dsk));
      r[e] = xx[e] + z;
    }
    u32x2 o = {pk2(r[0], r[1]), pk2(r[2], r[3])};
    if (valid) *(u32x2*)(outT + (size_t)d * L_ + l) = o;
  }
  STAMP(ws, 1, 4);
}

template <int MODE>
__global__ __launch_bounds__(MODE == 0 ? 704 : 640) void k_gemm(const u16* __restrict__ XT, const u32x4* __restrict__ Wp,
                                                                const u16* __restrict__ resT, u16* __restrict__ out,
                                                                char* __restrict__ ws, f32x2* __restrict__ stout) {
  constexpr int NT = (MODE == 0) ? 11 : 10;
  constexpr int NTH = NT * 64;
  __shared__ __attribute__((aligned(16))) char smem[40960 + 32 * 164 * 4];
  const int t = threadIdx.x, lane = t & 63, w = __builtin_amdgcn_readfirstlane(t >> 6);
  const int b = blockIdx.x;
  const int cg = (b >> 3) & 3, rb = (b & 7) + 8 * (b >> 5);
  const int l0 = rb * 32;
  const int g = lane >> 4, li = lane & 15;
  const int ntile = cg * NT + w;
  STAMP(ws, 2 + MODE, 0);
  u32x4 xv[4];
#pragma unroll
  for (int i = 0; i < 4; i++) {
    const int c = t + NTH * i;
    if (c < 2560) xv[i] = *(const u32x4*)(XT + (size_t)(c >> 2) * L_ + l0 + (c & 3) * 8);
  }
  const u32x4* wp0 = Wp + (size_t)ntile * 20 * 64 + lane;
  u32x4 wf[20];
#pragma unroll
  for (int p = 0; p < 3; p++) wf[p] = wp0[p * 64];
  __builtin_amdgcn_sched_barrier(0);
#pragma unroll
  for (int i = 0; i < 4; i++) {
    const int c = t + NTH * i;
    const int row = c >> 2;
    if (c < 2560) *(u32x4*)(smem + row * 64 + (((c & 3) ^ (2 * ((row >> 3) & 1))) * 16)) = xv[i];
  }
  __syncthreads();
  STAMP(ws, 2 + MODE, 1);
  f32x4 acc[2] = {{0, 0, 0, 0}, {0, 0, 0, 0}};
  const int q4 = li >> 2, p4 = li & 3;
  const unsigned sbase = (unsigned)(size_t)smem;
  unsigned xa[2];
#pragma unroll
  for (int lt = 0; lt < 2; lt++)
    xa[lt] = sbase + (8 * g + q4) * 64 + (((2 * lt + (p4 >> 1)) ^ (2 * (g & 1))) * 16) + 8 * (p4 & 1);
  f32x2* stp = (f32x2*)(smem + 40960);
  if (MODE == 0) {
    float ssum[2] = {0.f, 0.f}, ssq[2] = {0.f, 0.f};
    const bf16x2 ones = __builtin_bit_cast(bf16x2, 0x3F803F80u);
#pragma unroll
    for (int kk = 0; kk < 2; kk++) {
      const int ks = w + NT * kk;
      if (ks < 20) {
#pragma unroll
        for (int lt = 0; lt < 2; lt++) {
          const u32x4 xf = tr_frag(xa[lt] + ks * 2048, xa[lt] + ks * 2048 + 256);
#pragma unroll
          for (int e = 0; e < 4; e++) {
            const unsigned ux = xf[e];
            bf16x2 xx = __builtin_bit_cast(bf16x2, ux);
            ssum[lt] = __builtin_amdgcn_fdot2_f32_bf16(xx, ones, ssum[lt], false);
            ssq[lt] = __builtin_amdgcn_fdot2_f32_bf16(xx, xx, ssq[lt], false);
          }
        }
      }
    }
#pragma unroll
    for (int lt = 0; lt < 2; lt++) {
      ssum[lt] += __shfl_xor(ssum[lt], 16); ssum[lt] += __shfl_xor(ssum[lt], 32);
      ssq[lt] += __shfl_xor(ssq[lt], 16); ssq[lt] += __shfl_xor(ssq[lt], 32);
      if (g == 0) { f32x2 o = {ssum[lt], ssq[lt]}; stp[w * 32 + 16 * lt + li] = o; }
    }
  }
  __builtin_amdgcn_sched_barrier(0);
#pragma unroll
  for (int p = 3; p < 20; p++) wf[p] = wp0[p * 64];
  f32x4 c4;
  u32x2 xr[2];
  u16 xs[2][4];
  if (MODE == 0) c4 = *(const f32x4*)((const float*)(ws + OFF_CS) + ntile * 16 + 4 * g);
  if (MODE == 1) {
#pragma unroll
    for (int lt = 0; lt < 2; lt++) xr[lt] = *(const u32x2*)(resT + (size_t)(ntile * 16 + li) * L_ + l0 + 16 * lt + 4 * g);
  }
  if (MODE == 2) {
#pragma unroll
    for (int lt = 0; lt < 2; lt++)
#pragma unroll
      for (int e = 0; e < 4; e++) xs[lt][e] = resT[(size_t)(ntile * 16 + 4 * g + e) * L_ + l0 + 16 * lt + li];
  }
#pragma unroll
  for (int ks = 0; ks < 20; ks++) {
    u32x4 xf[2];
#pragma unroll
    for (int lt = 0; lt < 2; lt++) xf[lt] = tr_frag(xa[lt] + ks * 2048, xa[lt] + ks * 2048 + 256);
    const bf16x8 wv = __builtin_bit_cast(bf16x8, wf[ks]);
#pragma unroll
    for (int lt = 0; lt < 2; lt++) {
      if (MODE == 1) acc[lt] = __builtin_amdgcn_mfma_f32_16x16x32_bf16(__builtin_bit_cast(bf16x8, xf[lt]), wv, acc[lt], 0, 0, 0);
      else acc[lt] = __builtin_amdgcn_mfma_f32_16x16x32_bf16(wv, __builtin_bit_cast(bf16x8, xf[lt]), acc[lt], 0, 0, 0);
    }
  }
  STAMP(ws, 2 + MODE, 2);
  if (MODE == 0) {
    __syncthreads();
    f32x2 svv[2];
#pragma unroll
    for (int lt = 0; lt < 2; lt++) {
      f32x2 a = {0.f, 0.f};
#pragma unroll
      for (int i = 0; i < 11; i++) a += stp[i * 32 + 16 * lt + li];
      float mean = a[0] * (1.0f / 640.0f);
      float var = fmaxf(a[1] * (1.0f / 640.0f) - mean * mean, 0.f);
      float sd = rbf(__builtin_amdgcn_sqrtf(rbf(rbf(var) + 1e-5f)));
      svv[lt] = (f32x2){rbf(mean), __builtin_amdgcn_rcpf(sd)};
    }
    u16* qo = (u16*)(ws + OFF_Q);
    char* kvimg = ws + OFF_KV;
    const int n0 = ntile * 16 + 4 * g;
#pragma unroll
    for (int lt = 0; lt < 2; lt++) {
      const f32x2 sv = svv[lt];
      const int l = l0 + 16 * lt + li;
      float v[4];
#pragma unroll
      for (int r = 0; r < 4; r++) v[r] = (acc[lt][r] - sv[0] * c4[r]) * sv[1];
      if (n0 < 640) {
        u32x2 o = {pk2(v[0] * QSCALE, v[1] * QSCALE), pk2(v[2] * QSCALE, v[3] * QSCALE)};
        *(u32x2*)(qo + (size_t)l * D_ + n0) = o;
      } else if (n0 < 660) {
        const int dh = n0 - 640;
        u32x2 o = {pk2(v[0], v[1]), pk2(v[2], v[3])};
        const int tile = l >> 5, step = dh >> 4, ln = (l & 31) + 32 * ((dh & 15) >> 3);
        *(u32x2*)(kvimg + tile * 4096 + step * 1024 + ln * 16 + (dh & 7) * 2) = o;
      } else if (n0 < 680) {
        const int dv = n0 - 660;
        const int tile = l >> 5, kk = l & 15, step = (l & 31) >> 4;
        const int hh = (kk & 7) >> 2, jj = (kk >> 3) * 4 + (kk & 3);
#pragma unroll
        for (int r = 0; r < 4; r++)
          *(u16*)(kvimg + tile * 4096 + 2048 + step * 1024 + (dv + r + 32 * hh) * 16 + jj * 2) = f2bf(v[r]);
      }
    }
  } else if (MODE == 1) {
    float* stg = (float*)(smem + 40960);
    const int nl = w * 16 + li;
    const int n = cg * 160 + nl;
#pragma unroll
    for (int lt = 0; lt < 2; lt++) {
      const int ll = 16 * lt + 4 * g;
      float r[4];
      r[0] = rbf(lo16(xr[lt][0]) + rbf(acc[lt][0]));
      r[1] = rbf(hi16(xr[lt][0]) + rbf(acc[lt][1]));
      r[2] = rbf(lo16(xr[lt][1]) + rbf(acc[lt][2]));
      r[3] = rbf(hi16(xr[lt][1]) + rbf(acc[lt][3]));
      u32x2 o = {pk2(r[0], r[1]), pk2(r[2], r[3])};
      *(u32x2*)(out + (size_t)n * L_ + l0 + ll) = o;
#pragma unroll
      for (int e = 0; e < 4; e++) stg[(ll + e) * 164 + nl] = r[e];
    }
    __syncthreads();
    if (t < 256) {
      const int row = t >> 3, part = t & 7;
      float v[20];
      float s = 0.f;
#pragma unroll
      for (int i = 0; i < 5; i++) {
        f32x4 a = *(const f32x4*)(stg + row * 164 + part * 20 + 4 * i);
#pragma unroll
        for (int e = 0; e < 4; e++) { v[4 * i + e] = a[e]; s += a[e]; }
      }
      s += __shfl_xor(s, 1); s += __shfl_xor(s, 2); s += __shfl_xor(s, 4);
      const float mean = s * (1.0f / 160.0f);
      float m2 = 0.f;
#pragma unroll
      for (int i = 0; i < 20; i++) { float a = v[i] - mean; m2 += a * a; }
      m2 += __shfl_xor(m2, 1); m2 += __shfl_xor(m2, 2); m2 += __shfl_xor(m2, 4);
      if (part == 0) { f32x2 o = {mean, m2}; stout[cg * L_ + l0 + row] = o; }
    }
  } else {
    const int n0 = ntile * 16 + 4 * g;
#pragma unroll
    for (int lt = 0; lt < 2; lt++) {
      const int l = l0 + 16 * lt + li;
      float r[4];
#pragma unroll
      for (int e = 0; e < 4; e++) r[e] = bf2f(xs[lt][e]) + rbf(acc[lt][e]);
      u32x2 o = {pk2(r[0], r[1]), pk2(r[2], r[3])};
      *(u32x2*)(out + (size_t)l * D_ + n0) = o;
    }
  }
  STAMP(ws, 2 + MODE, 3);
}

__device__ __forceinline__ void pack_p(const f32x16& s, bf16x8& p0, bf16x8& p1) {
  u32x4 a, c;
#pragma unroll
  for (int i = 0; i < 4; i++) {
    a[i] = pk2(__builtin_amdgcn_exp2f(s[2 * i]), __builtin_amdgcn_exp2f(s[2 * i + 1]));
    c[i] = pk2(__builtin_amdgcn_exp2f(s[8 + 2 * i]), __builtin_amdgcn_exp2f(s[9 + 2 * i]));
  }
  p0 = __builtin_bit_cast(bf16x8, a);
  p1 = __builtin_bit_cast(bf16x8, c);
}
__device__ __forceinline__ float max16(const f32x16& s) {
  float m = fmaxf(s[0], s[1]);
#pragma unroll
  for (int i = 2; i < 16; i++) m = fmaxf(m, s[i]);
  return m;
}

__global__ __launch_bounds__(1024) void k_attn(const char* __restrict__ ws_q, const u32x4* __restrict__ kv,
                                               u16* __restrict__ oT) {
  __shared__ __attribute__((aligned(16))) u32x4 sm[3][2048];
  const int t = threadIdx.x, lane = t & 63, w = __builtin_amdgcn_readfirstlane(t >> 6);
  const int b = blockIdx.x;
  const int kh = w >> 3;
  const int pb = b >> 2, head = (b & 3) * 8 + (w & 7);
  const int ci = lane & 31, h = lane >> 5;
  const int l = pb * 32 + ci;
  STAMP(ws_q - OFF_Q, 5, 0);
  u32x4 stg[2], stg1[2];
  stg[0] = kv[t];
  stg[1] = kv[8192 + t];
  stg1[0] = kv[1024 + t];
  stg1[1] = kv[8192 + 1024 + t];
  bf16x8 q0, q1;
  {
    const char* qp = ws_q + (size_t)l * (D_ * 2) + head * 40;
    u32x2 a = *(const u32x2*)(qp + 16 * h), c = *(const u32x2*)(qp + 16 * h + 8);
    u32x2 e = *(const u32x2*)(qp + 32);
    u32x4 f0 = {a[0], a[1], c[0], c[1]};
    u32x4 f1 = {h ? 0u : e[0], h ? 0u : e[1], 0u, 0u};
    q0 = __builtin_bit_cast(bf16x8, f0);
    q1 = __builtin_bit_cast(bf16x8, f1);
  }
  sm[0][t] = stg[0];
  sm[0][1024 + t] = stg[1];
  sm[1][t] = stg1[0];
  sm[1][1024 + t] = stg1[1];
  __syncthreads();
  STAMP(ws_q - OFF_Q, 5, 1);
  STAMPC(ws_q - OFF_Q, 6, 0);
  f32x16 negm;
  {
    f32x16 z = {0};
    f32x16 s = __builtin_amdgcn_mfma_f32_32x32x16_bf16(__builtin_bit_cast(bf16x8, sm[0][lane]), q0, z, 0, 0, 0);
    s = __builtin_amdgcn_mfma_f32_32x32x16_bf16(__builtin_bit_cast(bf16x8, sm[0][64 + lane]), q1, s, 0, 0, 0);
    float m = max16(s);
    m = fmaxf(m, __shfl_xor(m, 32));
#pragma unroll
    for (int i = 0; i < 16; i++) negm[i] = -m;
  }
  f32x16 o = {0};
#define SB() __builtin_amdgcn_sched_barrier(0)
  const u32x4* base = &sm[0][0] + kh * 1024 + lane;
  u32x4 k0n = base[0], k1n = base[64];
  u32x4 v0 = base[128], v1 = base[192];
  f32x16 s = __builtin_amdgcn_mfma_f32_32x32x16_bf16(__builtin_bit_cast(bf16x8, k0n), q0, negm, 0, 0, 0);
  s = __builtin_amdgcn_mfma_f32_32x32x16_bf16(__builtin_bit_cast(bf16x8, k1n), q1, s, 0, 0, 0);
  k0n = base[256]; k1n = base[256 + 64];
  int bi = 0;
  for (int c = 0; c < 8; c++) {
    const int bn = (bi == 2) ? 0 : bi + 1, bw = (bn == 2) ? 0 : bn + 1;
    const u32x4* buf = base + bi * 2048;
    const u32x4* nbuf = base + bn * 2048;
    if (c + 2 < 8) {
      stg[0] = kv[(c + 2) * 1024 + t];
      stg[1] = kv[8192 + (c + 2) * 1024 + t];
    }
    SB();
#pragma unroll
    for (int tl = 0; tl < 4; tl++) {
      const u32x4* kp = (tl < 2) ? buf + (tl + 2) * 256 : nbuf + (tl - 2) * 256;
      const u32x4* vp = (tl < 3) ? buf + (tl + 1) * 256 : nbuf;
      u32x4 pa, pb;
      if (tl == 0) __builtin_amdgcn_s_setprio(3);
      else if (tl == 1) __builtin_amdgcn_s_setprio(2);
      else if (tl == 2) __builtin_amdgcn_s_setprio(1);
      else __builtin_amdgcn_s_setprio(0);
      f32x16 sn = __builtin_amdgcn_mfma_f32_32x32x16_bf16(__builtin_bit_cast(bf16x8, k0n), q0, negm, 0, 0, 0);
      SB();
      pa[0] = pk2(__builtin_amdgcn_exp2f(s[0]), __builtin_amdgcn_exp2f(s[1]));
      pa[1] = pk2(__builtin_amdgcn_exp2f(s[2]), __builtin_amdgcn_exp2f(s[3]));
      SB();
      sn = __builtin_amdgcn_mfma_f32_32x32x16_bf16(__builtin_bit_cast(bf16x8, k1n), q1, sn, 0, 0, 0);
      k0n = kp[0]; k1n = kp[64];
      SB();
      pa[2] = pk2(__builtin_amdgcn_exp2f(s[4]), __builtin_amdgcn_exp2f(s[5]));
      pa[3] = pk2(__builtin_amdgcn_exp2f(s[6]), __builtin_amdgcn_exp2f(s[7]));
      pb[0] = pk2(__builtin_amdgcn_exp2f(s[8]), __builtin_amdgcn_exp2f(s[9]));
      SB();
      o = __builtin_amdgcn_mfma_f32_32x32x16_bf16(__builtin_bit_cast(bf16x8, v0), __builtin_bit_cast(bf16x8, pa), o, 0, 0, 0);
      v0 = vp[128];
      SB();
      pb[1] = pk2(__builtin_amdgcn_exp2f(s[10]), __builtin_amdgcn_exp2f(s[11]));
      pb[2] = pk2(__builtin_amdgcn_exp2f(s[12]), __builtin_amdgcn_exp2f(s[13]));
      pb[3] = pk2(__builtin_amdgcn_exp2f(s[14]), __builtin_amdgcn_exp2f(s[15]));
      SB();
      o = __builtin_amdgcn_mfma_f32_32x32x16_bf16(__builtin_bit_cast(bf16x8, v1), __builtin_bit_cast(bf16x8, pb), o, 0, 0, 0);
      v1 = vp[192];
      s = sn;
      SB();
    }
    if (c + 2 < 8) {
      sm[bw][t] = stg[0];
      sm[bw][1024 + t] = stg[1];
    }
    __syncthreads();
    bi = bn;
  }
  STAMP(ws_q - OFF_Q, 5, 2);
  STAMPC(ws_q - OFF_Q, 6, 1);
  f32x4* mg = (f32x4*)&sm[0][0];
  if (kh == 1) {
#pragma unroll
    for (int i = 0; i < 3; i++) {
      f32x4 a = {o[4 * i], o[4 * i + 1], o[4 * i + 2], o[4 * i + 3]};
      mg[((w & 7) * 3 + i) * 64 + lane] = a;
    }
  }
  __syncthreads();
  if (kh == 1) return;
#pragma unroll
  for (int i = 0; i < 3; i++) {
    f32x4 a = mg[(w * 3 + i) * 64 + lane];
#pragma unroll
    for (int e = 0; e < 4; e++) o[4 * i + e] += a[e];
  }
  float lsum = __shfl(o[8], ci + 32);
  bool bad = !(lsum <= 1.8446744e19f);
#pragma unroll
  for (int i = 0; i < 12; i++) bad = bad || !(fabsf(o[i]) <= 3.0e38f);
  if (__any(bad)) {
    float m = -INFINITY;
#pragma unroll
    for (int i = 0; i < 16; i++) o[i] = 0.f;
    for (int tl = 0; tl < 64; tl++) {
      f32x16 z = {0};
      f32x16 s = __builtin_amdgcn_mfma_f32_32x32x16_bf16(__builtin_bit_cast(bf16x8, kv[tl * 256 + lane]), q0, z, 0, 0, 0);
      s = __builtin_amdgcn_mfma_f32_32x32x16_bf16(__builtin_bit_cast(bf16x8, kv[tl * 256 + 64 + lane]), q1, s, 0, 0, 0);
      float tm = max16(s);
      tm = fmaxf(tm, __shfl_xor(tm, 32));
      const float mn = fmaxf(m, tm);
      const float al = __builtin_amdgcn_exp2f(m - mn);
      m = mn;
#pragma unroll
      for (int i = 0; i < 16; i++) { o[i] *= al; s[i] -= mn; }
      bf16x8 p0, p1;
      pack_p(s, p0, p1);
      o = __builtin_amdgcn_mfma_f32_32x32x16_bf16(__builtin_bit_cast(bf16x8, kv[tl * 256 + 128 + lane]), p0, o, 0, 0, 0);
      o = __builtin_amdgcn_mfma_f32_32x32x16_bf16(__builtin_bit_cast(bf16x8, kv[tl * 256 + 192 + lane]), p1, o, 0, 0, 0);
    }
    lsum = __shfl(o[8], ci + 32);
  }
  const float inv = 1.0f / lsum;
  u16* op = oT + (size_t)(head * 20 + 4 * h) * L_ + l;
#pragma unroll
  for (int i = 0; i < 12; i++) {
    const int dd = (i & 3) + 8 * (i >> 2);
    if (i < 8 || h == 0) op[(size_t)dd * L_] = f2bf(o[i] * inv);
  }
  STAMP(ws_q - OFF_Q, 5, 3);
}

extern "C" void kernel_launch(void* const* d_in, const int* in_sizes, int n_in, void* d_out, int out_size,
                              void* d_ws, size_t ws_size, hipStream_t stream) {
  const u16* x = (const u16*)d_in[0];
  const u16* logA = (const u16*)d_in[1];
  const u16* Aim = (const u16*)d_in[2];
  const u16* Cre = (const u16*)d_in[3];
  const u16* Cim = (const u16*)d_in[4];
  const u16* logdt = (const u16*)d_in[5];
  const u16* dskip = (const u16*)d_in[6];
  const u16* wq = (const u16*)d_in[8];
  const u16* wk = (const u16*)d_in[9];
  const u16* wv = (const u16*)d_in[10];
  const u16* wo = (const u16*)d_in[11];
  char* ws = (char*)d_ws;
  u16* xt0 = (u16*)(ws + OFF_XT0);
  u16* x1t = (u16*)(ws + OFF_X1T);
  u16* x2t = (u16*)(ws + OFF_X2T);
  u16* x3t = (u16*)(ws + OFF_X3T);
  u16* ot = (u16*)(ws + OFF_OT);
  const u32x4* wqkv = (const u32x4*)(ws + OFF_WQKV);
  const u32x4* wop = (const u32x4*)(ws + OFF_WO);
  const u32x4* kv = (const u32x4*)(ws + OFF_KV);
  f32x2* st0 = (f32x2*)(ws + OFF_ST0);
  f32x2* st1 = (f32x2*)(ws + OFF_ST1);

  k_prep<<<PREP_WGS, 1024, 0, stream>>>(x, logA, Aim, Cre, Cim, logdt, wq, wk, wv, wo, ws);
  k_conv<<<214, 768, 0, stream>>>(xt0, st0, ws, dskip, x1t);
  k_gemm<0><<<256, 704, 0, stream>>>(x1t, wqkv, nullptr, nullptr, ws, nullptr);
  k_attn<<<256, 1024, 0, stream>>>(ws + OFF_Q, kv, ot);
  k_gemm<1><<<256, 640, 0, stream>>>(ot, wop, x1t, x2t, ws, st1);
  k_conv<<<214, 768, 0, stream>>>(x2t, st1, ws, dskip, x3t);
  k_gemm<0><<<256, 704, 0, stream>>>(x3t, wqkv, nullptr, nullptr, ws, nullptr);
  k_attn<<<256, 1024, 0, stream>>>(ws + OFF_Q, kv, ot);
  k_gemm<2><<<256, 640, 0, stream>>>(ot, wop, x3t, (u16*)d_out, ws, nullptr);
}
```

```cpp
#include <hip/hip_runtime.h>
#include <hip/hip_bf16.h>
#include <stdint.h>

typedef unsigned short u16;
typedef __attribute__((ext_vector_type(8))) __bf16 bf16x8;
typedef __attribute__((ext_vector_type(8))) _Float16 f16x8;
typedef __attribute__((ext_vector_type(16))) float f32x16;
typedef __attribute__((ext_vector_type(4))) float f32x4;
typedef __attribute__((ext_vector_type(4))) unsigned u32x4;
typedef __attribute__((ext_vector_type(2))) unsigned u32x2;
typedef __attribute__((ext_vector_type(4))) short s16x4;
typedef __attribute__((ext_vector_type(2))) float f32x2;

#define LDS_AS __attribute__((address_space(3)))
#define STAMP(wsp, kid, i)
#define STAMPC(wsp, kid, i)

constexpr int L_ = 2048, D_ = 640, NS_ = 128;
constexpr int KFROW = 2304;
constexpr int KV_TB = 2624, KV_K1 = 1024, KV_V0 = 1280, KV_V1 = 1952;
constexpr float LOG2E = 1.4426950408889634f;
constexpr float INV2PI = 0.15915494309189535f;
constexpr float QSCALE = 0.22360679774997896f * 1.4426950408889634f;

constexpr size_t SZ_ACT = (size_t)D_ * L_ * 2;
constexpr size_t OFF_XT0 = 0;
constexpr size_t OFF_X1T = OFF_XT0 + SZ_ACT;
constexpr size_t OFF_X2T = OFF_X1T + SZ_ACT;
constexpr size_t OFF_X3T = OFF_X2T + SZ_ACT;
constexpr size_t OFF_Q = OFF_X3T + SZ_ACT;
constexpr size_t OFF_OT = OFF_Q + SZ_ACT;
constexpr size_t OFF_KF = OFF_OT + SZ_ACT;
constexpr size_t OFF_KINV = OFF_KF + (size_t)D_ * KFROW * 2;
constexpr size_t OFF_WQKV = OFF_KINV + 4096;
constexpr size_t OFF_WO = OFF_WQKV + (size_t)44 * 20 * 1024;
constexpr size_t OFF_CS = OFF_WO + (size_t)40 * 20 * 1024;
constexpr size_t OFF_KV = OFF_CS + 4096;
constexpr size_t OFF_ST0 = OFF_KV + 262144;
constexpr size_t OFF_ST1 = OFF_ST0 + 65536;

__device__ __forceinline__ float bf2f(u16 v) { return __uint_as_float(((unsigned)v) << 16); }
__device__ __forceinline__ u16 f2bf(float f) { __bf16 b = (__bf16)f; return __builtin_bit_cast(u16, b); }
__device__ __forceinline__ float rbf(float f) { return bf2f(f2bf(f)); }
typedef __attribute__((ext_vector_type(2))) __bf16 bf16x2;
__device__ __forceinline__ unsigned pk2(float a, float b) {
  f32x2 v = {a, b};
  return __builtin_bit_cast(unsigned, __builtin_convertvector(v, bf16x2));
}
__device__ __forceinline__ float lo16(unsigned v) { return __uint_as_float(v << 16); }
__device__ __forceinline__ float hi16(unsigned v) { return __uint_as_float(v & 0xffff0000u); }

constexpr int PREP_WGS = 256;
constexpr int PREP_UNIT_LDS = 23808;

__device__ __forceinline__ int hi_value(int idx) {
  int v;
  if (idx < 16) v = 128 * (idx >> 3) + (idx & 7);
  else if (idx < 24) v = 256 + 128 * ((idx - 16) >> 2) + 2 * ((idx - 16) & 3);
  else if (idx < 32) v = 512 + 128 * ((idx - 24) >> 1) + 4 * ((idx - 24) & 1);
  else if (idx <= 40) v = 1024 + 128 * (idx - 32);
  else v = 0;
  return v;
}

__device__ __forceinline__ u32x4 tr_frag(unsigned addr0, unsigned addr1) {
  s16x4 a = __builtin_amdgcn_ds_read_tr16_b64_v4i16((LDS_AS s16x4*)(size_t)addr0);
  s16x4 c = __builtin_amdgcn_ds_read_tr16_b64_v4i16((LDS_AS s16x4*)(size_t)addr1);
  u32x2 a2 = __builtin_bit_cast(u32x2, a), c2 = __builtin_bit_cast(u32x2, c);
  return (u32x4){a2[0], a2[1], c2[0], c2[1]};
}
__device__ __forceinline__ void split2(float a, float b, unsigned& hi, unsigned& lo) {
  hi = __builtin_bit_cast(unsigned, __builtin_amdgcn_cvt_pkrtz(a, b));
  float la, lb;
  asm("v_fma_mix_f32 %0, %1, -1.0, %2 op_sel_hi:[1,0,0]" : "=v"(la) : "v"(hi), "v"(a));
  asm("v_fma_mix_f32 %0, %1, -1.0, %2 op_sel:[1,0,0] op_sel_hi:[1,0,0]" : "=v"(lb) : "v"(hi), "v"(b));
  lo = __builtin_bit_cast(unsigned, __builtin_amdgcn_cvt_pkrtz(la, lb));
}

__global__ __launch_bounds__(1024) void k_prep(const u16* __restrict__ x, const u16* __restrict__ logA,
                                               const u16* __restrict__ Aim, const u16* __restrict__ Cre,
                                               const u16* __restrict__ Cim, const u16* __restrict__ logdt,
                                               const u16* __restrict__ wq, const u16* __restrict__ wk,
                                               const u16* __restrict__ wv, const u16* __restrict__ wo,
                                               char* __restrict__ ws) {
  __shared__ __attribute__((aligned(16))) char smem_all[4 * PREP_UNIT_LDS];
  const int ug = __builtin_amdgcn_readfirstlane(threadIdx.x >> 8);
  char* smem = smem_all + ug * PREP_UNIT_LDS;
  const int t = threadIdx.x & 255, lane = t & 63, w = __builtin_amdgcn_readfirstlane(t >> 6);
  const unsigned sbase = (unsigned)(size_t)smem;
  const int g = lane >> 4, li = lane & 15, q4 = li >> 2, p4 = li & 3;
  const int b = blockIdx.x;
  if (ug < 3 && b >= 214) {
    __syncthreads(); __syncthreads(); __syncthreads();
  } else if (ug < 3) {
    const int d = min(b * 3 + ug, D_ - 1);
    STAMP(ws, 0, 0);
    float* prm = (float*)smem;
    float* part = prm + 512;
    float* tab = part + 4 * 768;
    float* red = tab + 2312;
    if (t < 128) {
      const float dt = rbf(expf(bf2f(logdt[d])));
      float ar = -rbf(expf(bf2f(logA[d * NS_ + t])));
      float alpha = ar * dt, beta = bf2f(Aim[d * NS_ + t]) * dt;
      const float cr = 2.0f * (bf2f(Cre[d * NS_ + t]) * dt), ci = 2.0f * (bf2f(Cim[d * NS_ + t]) * dt);
      float bnd = hypotf(cr, ci);
      f32x4 pv = {alpha * LOG2E, beta * INV2PI, log2f(bnd), atan2f(ci, cr) * INV2PI};
      *(f32x4*)(prm + t * 4) = pv;
#pragma unroll
      for (int o = 32; o > 0; o >>= 1) bnd += __shfl_xor(bnd, o);
      if (lane == 0) red[w] = bnd;
    }
    __syncthreads();
    STAMP(ws, 0, 1);
    const float bnd = red[0] + red[1];
    int ex = (int)((__float_as_uint(bnd) >> 23) & 0xff) - 126;
    if (!(bnd > 1e-30f && bnd < 1e30f)) ex = 15;
    const float sA = (float)(14 - ex);
    const float lo = 8.0f * li;
    float hv[3] = {(float)hi_value(li), (float)hi_value(li + 16), (float)hi_value(li + 32)};
    f32x4 acc[3] = {{0, 0, 0, 0}, {0, 0, 0, 0}, {0, 0, 0, 0}};
#pragma unroll 1
    for (int ks = 0; ks < 2; ks++) {
      u32x4 bh, bl, ah[3], al[3];
#pragma unroll
      for (int m = 0; m < 4; m++) {
        const int n = 32 * w + 16 * ks + 4 * g + m;
        f32x4 p = *(const f32x4*)(prm + n * 4);
        p[2] += sA;
        {
          float e = __builtin_amdgcn_exp2f(fmaf(p[0], lo, 14.0f));
          float ph = __builtin_amdgcn_fractf(p[1] * lo);
          unsigned h_, l_;
          split2(e * __builtin_amdgcn_cosf(ph), e * __builtin_amdgcn_sinf(ph), h_, l_);
          bh[m] = h_; bl[m] = l_;
        }
#pragma unroll
        for (int tt = 0; tt < 3; tt++) {
          float e2 = __builtin_amdgcn_exp2f(fmaf(p[0], hv[tt], p[2]));
          float ph2 = __builtin_amdgcn_fractf(fmaf(p[1], hv[tt], p[3]));
          unsigned h_, l_;
          split2(e2 * __builtin_amdgcn_cosf(ph2), -e2 * __builtin_amdgcn_sinf(ph2), h_, l_);
          ah[tt][m] = h_; al[tt][m] = l_;
        }
      }
#pragma unroll
      for (int tt = 0; tt < 3; tt++) {
        acc[tt] = __builtin_amdgcn_mfma_f32_16x16x32_f16(__builtin_bit_cast(f16x8, al[tt]), __builtin_bit_cast(f16x8, bh), acc[tt], 0, 0, 0);
        acc[tt] = __builtin_amdgcn_mfma_f32_16x16x32_f16(__builtin_bit_cast(f16x8, ah[tt]), __builtin_bit_cast(f16x8, bl), acc[tt], 0, 0, 0);
        acc[tt] = __builtin_amdgcn_mfma_f32_16x16x32_f16(__builtin_bit_cast(f16x8, ah[tt]), __builtin_bit_cast(f16x8, bh), acc[tt], 0, 0, 0);
      }
    }
    STAMP(ws, 0, 2);
#pragma unroll
    for (int tt = 0; tt < 3; tt++)
#pragma unroll
      for (int r = 0; r < 4; r++) part[w * 768 + (16 * tt + g * 4 + r) * 16 + li] = acc[tt][r];
    __syncthreads();
#pragma unroll
    for (int k = 0; k < 3; k++) {
      const int i = t + 256 * k;
      const int hi = i >> 4, lo_i = i & 15;
      if (hi < 40 || (hi == 40 && lo_i == 0))
      {
        const int pp = hi_value(hi) + 8 * lo_i;
        tab[pp + (pp >> 3)] = (part[i] + part[768 + i]) + (part[1536 + i] + part[2304 + i]);
      }
    }
    __syncthreads();
    STAMP(ws, 0, 3);
    const float sc = 1.0f / 8192.0f;
    f16x8 o;
#pragma unroll
    for (int e = 0; e < 8; e++) {
      const int pp = (int)rbf((float)(8 * t + e));
      o[e] = (_Float16)(tab[pp + (pp >> 3)] * sc);
    }
    f16x8* dst = (f16x8*)(ws + OFF_KF + (size_t)d * KFROW * 2);
    dst[31 + t] = o;
    if (t < 32) {
      f16x8 z = {0, 0, 0, 0, 0, 0, 0, 0};
      dst[t < 31 ? t : 287] = z;
    }
    if (t == 0) ((float*)(ws + OFF_KINV))[d] = __uint_as_float((unsigned)(127 + ex - 15 - 9) << 23);
    STAMP(ws, 0, 4);
  } else if (b < 128) {
    __syncthreads();
    const int l0 = b * 16, row = t >> 4, part = t & 15;
    const u32x4* src = (const u32x4*)(x + (size_t)(l0 + row) * D_ + part * 40);
    u32x4 v[5];
#pragma unroll
    for (int i = 0; i < 5; i++) v[i] = src[i];
    if (t < 2) {
      const int c = b * 2 + t;
      const int tile = c >> 2, step = (c >> 1) & 1, hh = c & 1;
      u32x4 o = {0x3F803F80u, 0x3F803F80u, 0x3F803F80u, 0x3F803F80u};
      *(u32x4*)(ws + OFF_KV + tile * KV_TB + KV_V0 + step * 672 + hh * 336 + 320) = o;
    }
    float s = 0.f;
#pragma unroll
    for (int i = 0; i < 5; i++)
#pragma unroll
      for (int j = 0; j < 4; j++) s += lo16(v[i][j]) + hi16(v[i][j]);
#pragma unroll
    for (int o = 1; o < 16; o <<= 1) s += __shfl_xor(s, o);
    const float mean = s * (1.0f / 640.0f);
    float m2 = 0.f;
#pragma unroll
    for (int i = 0; i < 5; i++)
#pragma unroll
      for (int j = 0; j < 4; j++) {
        float a = lo16(v[i][j]) - mean, c = hi16(v[i][j]) - mean;
        m2 += a * a + c * c;
      }
#pragma unroll
    for (int o = 1; o < 16; o <<= 1) m2 += __shfl_xor(m2, o);
    if (part == 0) {
      const float vb = rbf(m2 * (1.0f / 640.0f));
      ((unsigned*)(ws + OFF_ST0))[l0 + row] = pk2(mean, __builtin_amdgcn_sqrtf(rbf(vb + 1e-5f)));
    }
#pragma unroll
    for (int i = 0; i < 5; i++) *(u32x4*)(smem + row * 1296 + part * 80 + i * 16) = v[i];
    __syncthreads();
    u16* xt = (u16*)(ws + OFF_XT0);
#pragma unroll
    for (int i = 0; i < 5; i++) {
      const int blk = (w * 5 + i) * 4 + g;
      const int ct = blk >> 1, rh = blk & 1;
      const unsigned ad = sbase + (rh * 8 + q4) * 1296 + ct * 32 + p4 * 8;
      const u32x4 f = tr_frag(ad, ad + 4 * 1296);
      *(u32x4*)(xt + (size_t)(ct * 16 + li) * L_ + l0 + rh * 8) = f;
    }
    __syncthreads();
  } else if (b < 208) {
    __syncthreads();
    const int u = b - 128;
    const int hf = u & 1, uu = u >> 1;
    const bool isq = uu < 20;
    const int ks = isq ? uu : uu - 20;
    const u16* W = isq ? wq : wo;
    u32x4* dst = (u32x4*)(ws + (isq ? OFF_WQKV : OFF_WO));
    u32x4 v[5];
#pragma unroll
    for (int i = 0; i < 5; i++) {
      const int c = t + 256 * i;
      const int r = c / 40, cc = c - r * 40;
      v[i] = *(const u32x4*)(W + (size_t)(ks * 32 + r) * 640 + hf * 320 + cc * 8);
    }
    const bool dokv = isq && hf == 0;
    u16 e[8];
    const int nkv = (40 + w) * 16 + li;
    if (dokv) {
#pragma unroll
      for (int j = 0; j < 8; j++) {
        const int k = ks * 32 + 8 * g + j;
        e[j] = (nkv < 660) ? wk[k * 20 + (nkv - 640)] : (nkv < 680) ? wv[k * 20 + (nkv - 660)] : (u16)0;
      }
    }
#pragma unroll
    for (int i = 0; i < 5; i++) {
      const int c = t + 256 * i;
      const int r = c / 40, cc = c - r * 40;
      *(u32x4*)(smem + r * 656 + cc * 16) = v[i];
    }
    __syncthreads();
#pragma unroll
    for (int i = 0; i < 5; i++) {
      const int nt = w * 5 + i;
      const unsigned ad = sbase + (8 * g + q4) * 656 + nt * 32 + p4 * 8;
      const u32x4 f = tr_frag(ad, ad + 4 * 656);
      dst[((size_t)(hf * 20 + nt) * 20 + ks) * 64 + lane] = f;
    }
    if (dokv) {
      u32x4 o;
#pragma unroll
      for (int j = 0; j < 4; j++) o[j] = (unsigned)e[2 * j] | ((unsigned)e[2 * j + 1] << 16);
      dst[((size_t)(40 + w) * 20 + ks) * 64 + lane] = o;
    }
    __syncthreads();
  } else if (b < 214) {
    __syncthreads(); __syncthreads(); __syncthreads();
  } else {
    __syncthreads();
    const int u = min(b - 214, 40);
    float sacc[8] = {0, 0, 0, 0, 0, 0, 0, 0};
    float* red = (float*)smem;
    if (u < 40) {
      const int c2 = t & 1, rg = t >> 1;
      const u32x4* src = (const u32x4*)(wq + (size_t)(rg * 5) * 640 + u * 16 + c2 * 8);
      u32x4 v[5];
#pragma unroll
      for (int i = 0; i < 5; i++) v[i] = src[i * 80];
#pragma unroll
      for (int i = 0; i < 5; i++)
#pragma unroll
        for (int j = 0; j < 4; j++) { sacc[2 * j] += lo16(v[i][j]); sacc[2 * j + 1] += hi16(v[i][j]); }
#pragma unroll
      for (int j = 0; j < 8; j++)
#pragma unroll
        for (int o = 2; o < 64; o <<= 1) sacc[j] += __shfl_xor(sacc[j], o);
      if (lane < 2) {
#pragma unroll
        for (int j = 0; j < 8; j++) red[w * 16 + lane * 8 + j] = sacc[j];
      }
    } else {
      const int c10 = t % 10, rg = t / 10;
      if (rg < 25) {
        const u16* src = ((c10 < 5) ? wk + c10 * 4 : wv + (c10 - 5) * 4);
#pragma unroll
        for (int i = 0; i < 26; i++) {
          const int row = rg + 25 * i;
          if (row < 640) {
            const u32x2 v = *(const u32x2*)(src + (size_t)row * 20);
            sacc[0] += lo16(v[0]); sacc[1] += hi16(v[0]); sacc[2] += lo16(v[1]); sacc[3] += hi16(v[1]);
          }
        }
#pragma unroll
        for (int j = 0; j < 4; j++) red[rg * 40 + c10 * 4 + j] = sacc[j];
      }
    }
    __syncthreads();
    if (u < 40) {
      if (t < 16) ((float*)(ws + OFF_CS))[u * 16 + t] = (red[t] + red[16 + t]) + (red[32 + t] + red[48 + t]);
    } else if (t < 64) {
      float v = 0.f;
      if (t < 40) {
#pragma unroll
        for (int i = 0; i < 25; i++) v += red[i * 40 + t];
      }
      ((float*)(ws + OFF_CS))[640 + t] = v;
    }
    __syncthreads();
  }
}

constexpr int CV_COPY = 321;
constexpr int CV_UOFF = 2576;
constexpr int CV_CH = CV_UOFF + 288;
constexpr int CV_ZERO = 3 * CV_CH;
constexpr int CV_STAT = CV_ZERO + 48;
constexpr int CV_TOTAL = CV_STAT + 512;

template <bool FINAL>
__global__ __launch_bounds__(768) void k_conv(const u16* __restrict__ xT, const f32x2* __restrict__ st,
                                              const char* __restrict__ ws, const u16* __restrict__ dskip,
                                              u16* __restrict__ outT) {
  __shared__ __attribute__((aligned(16))) u32x4 sm[CV_TOTAL];
  const int tg = threadIdx.x;
  const int cgp = __builtin_amdgcn_readfirstlane(tg >> 8), t = tg & 255, lane = t & 63;
  const int w = __builtin_amdgcn_readfirstlane(t >> 6);
  const int dreal = blockIdx.x * 3 + cgp;
  const bool valid = dreal < D_;
  const int d = valid ? dreal : D_ - 1;
  const int ci = lane & 31, h = lane >> 5;
  u32x4* cb = sm + cgp * CV_CH;
  STAMP(ws, 1, 0);
  const u32x4* kg = (const u32x4*)(ws + OFF_KF + (size_t)d * KFROW * 2);
  const u32x4 kc = kg[31 + t], kp = kg[30 + t];
  f32x2 sp[3][4];
  u32x4 sf0, sf1;
  if (FINAL) {
    sf0 = ((const u32x4*)st)[2 * t];
    sf1 = ((const u32x4*)st)[2 * t + 1];
  } else {
#pragma unroll
    for (int i = 0; i < 3; i++) {
      const int row = min(tg + 768 * i, L_ - 1);
#pragma unroll
      for (int p = 0; p < 4; p++) sp[i][p] = st[p * L_ + row];
    }
  }
  const u32x4 xv = *(const u32x4*)(xT + (size_t)d * L_ + 8 * t);
  const float kinv = ((const float*)(ws + OFF_KINV))[d];
  const int lo0 = 8 * ci + 4 * h + 256 * w;
  const u32x2 xo0 = *(const u32x2*)(xT + (size_t)d * L_ + lo0);
  const u32x2 xo1 = *(const u32x2*)(xT + (size_t)d * L_ + lo0 + 1024);
  const float dsk = bf2f(dskip[d]);
  {
    u32x4 z = {0, 0, 0, 0};
    if (tg < 48) sm[CV_ZERO + tg] = z;
    if (t < 32) cb[CV_UOFF + (t < 31 ? t : 287)] = z;
  }
  {
    unsigned Z[8], Y[7];
#pragma unroll
    for (int m = 0; m < 4; m++) {
      Z[m] = __builtin_amdgcn_alignbit(kc[3 - m], kc[3 - m], 16);
      Z[4 + m] = __builtin_amdgcn_alignbit(kp[3 - m], kp[3 - m], 16);
    }
#pragma unroll
    for (int m = 0; m < 7; m++) Y[m] = __builtin_amdgcn_alignbit(Z[m + 1], Z[m], 16);
    const int phys = (t >> 5) * 40 + (t & 31);
#pragma unroll
    for (int r = 0; r < 8; r++) {
      const int o = 7 - r;
      u32x4 v;
      if ((o & 1) == 0) { v[0] = Z[o / 2]; v[1] = Z[o / 2 + 1]; v[2] = Z[o / 2 + 2]; v[3] = Z[o / 2 + 3]; }
      else { v[0] = Y[o / 2]; v[1] = Y[o / 2 + 1]; v[2] = Y[o / 2 + 2]; v[3] = Y[o / 2 + 3]; }
      cb[r * CV_COPY + phys] = v;
    }
  }
  if (!FINAL) {
#pragma unroll
  for (int i = 0; i < 3; i++) {
    const int row = tg + 768 * i;
    float m0 = sp[i][0][0], m1 = sp[i][1][0], m2 = sp[i][2][0], m3 = sp[i][3][0];
    float mean = 0.25f * ((m0 + m1) + (m2 + m3));
    float a0 = m0 - mean, a1 = m1 - mean, a2 = m2 - mean, a3 = m3 - mean;
    float M2 = ((sp[i][0][1] + sp[i][1][1]) + (sp[i][2][1] + sp[i][3][1])) +
               160.0f * ((a0 * a0 + a1 * a1) + (a2 * a2 + a3 * a3));
    float vb = rbf(M2 * (1.0f / 640.0f));
    float sd = __builtin_amdgcn_sqrtf(rbf(vb + 1e-5f));
    if (row < L_) ((unsigned*)(sm + CV_STAT))[row] = pk2(mean, sd);
  }
  __syncthreads();
  }
  STAMP(ws, 1, 1);
  {
    const u32x4 s0 = FINAL ? sf0 : sm[CV_STAT + 2 * t], s1 = FINAL ? sf1 : sm[CV_STAT + 2 * t + 1];
    u32x4 c4;
#pragma unroll
    for (int i = 0; i < 4; i++) {
      const unsigned sa = i < 2 ? s0[2 * i] : s1[2 * i - 4], sb = i < 2 ? s0[2 * i + 1] : s1[2 * i - 3];
      float u0 = rbf(rbf(lo16(xv[i]) - lo16(sa)) * __builtin_amdgcn_rcpf(hi16(sa)));
      float u1 = rbf(rbf(hi16(xv[i]) - lo16(sb)) * __builtin_amdgcn_rcpf(hi16(sb)));
      _Float16 f0 = (_Float16)(u0 * 512.0f), f1 = (_Float16)(u1 * 512.0f);
      c4[i] = (unsigned)__builtin_bit_cast(u16, f0) | ((unsigned)__builtin_bit_cast(u16, f1) << 16);
    }
    cb[CV_UOFF + 31 + t] = c4;
  }
  __syncthreads();
  STAMP(ws, 1, 2);
  STAMPC(ws, 10, 0);
  f32x16 T0 = {0}, T1 = {0};
  {
    const int r = ci & 7, al = ci >> 3;
    const int blo = al - w, bhi = al + 4 - w;
    const int alo = (blo >= 0) ? (cgp * CV_CH + r * CV_COPY + 40 * blo + 1 - h) : (CV_ZERO + r + 8 * (blo & 1) + 1 - h);
    const int ahi = cgp * CV_CH + r * CV_COPY + 40 * bhi + 1 - h;
    const int ua = cgp * CV_CH + CV_UOFF + ci + h + 32 * w;
    u32x4 a_lo = sm[alo + 30], a_hi = sm[ahi + 30], b_a = sm[ua], b_b = sm[ua + 128];
#pragma unroll
    for (int s = 0; s < 16; s++) {
      u32x4 n_lo, n_hi, n_a, n_b;
      if (s < 15) {
        n_lo = sm[alo + 28 - 2 * s]; n_hi = sm[ahi + 28 - 2 * s];
        n_a = sm[ua + 2 * s + 2]; n_b = sm[ua + 2 * s + 130];
      }
      __builtin_amdgcn_sched_barrier(0);
      T0 = __builtin_amdgcn_mfma_f32_32x32x16_f16(__builtin_bit_cast(f16x8, a_lo), __builtin_bit_cast(f16x8, b_a), T0, 0, 0, 0);
      T1 = __builtin_amdgcn_mfma_f32_32x32x16_f16(__builtin_bit_cast(f16x8, a_hi), __builtin_bit_cast(f16x8, b_a), T1, 0, 0, 0);
      T1 = __builtin_amdgcn_mfma_f32_32x32x16_f16(__builtin_bit_cast(f16x8, a_lo), __builtin_bit_cast(f16x8, b_b), T1, 0, 0, 0);
      __builtin_amdgcn_sched_barrier(0);
      if (s < 15) { a_lo = n_lo; a_hi = n_hi; b_a = n_a; b_b = n_b; }
    }
  }
  STAMP(ws, 1, 3);
  __syncthreads();
  STAMPC(ws, 10, 1);
  STAMP(ws, 1, 5);
  f32x4* redc = (f32x4*)cb;
#pragma unroll
  for (int g = 0; g < 4; g++) {
    f32x4 a = {T0[4 * g], T0[4 * g + 1], T0[4 * g + 2], T0[4 * g + 3]};
    f32x4 c = {T1[4 * g], T1[4 * g + 1], T1[4 * g + 2], T1[4 * g + 3]};
    redc[((w * 2 + 0) * 4 + g) * 64 + lane] = a;
    redc[((w * 2 + 1) * 4 + g) * 64 + lane] = c;
  }
  __syncthreads();
#pragma unroll
  for (int T = 0; T < 2; T++) {
    f32x4 y = (redc[((0 * 2 + T) * 4 + w) * 64 + lane] + redc[((1 * 2 + T) * 4 + w) * 64 + lane]) +
              (redc[((2 * 2 + T) * 4 + w) * 64 + lane] + redc[((3 * 2 + T) * 4 + w) * 64 + lane]);
    const int l = lo0 + 1024 * T;
    const u32x2 ur = *((const u32x2*)(cb + CV_UOFF + 31 + (l >> 3)) + h);
    float uu[4];
    uu[0] = (float)__builtin_bit_cast(_Float16, (u16)(ur[0] & 0xffffu)) * (1.0f / 512.0f);
    uu[1] = (float)__builtin_bit_cast(_Float16, (u16)(ur[0] >> 16)) * (1.0f / 512.0f);
    uu[2] = (float)__builtin_bit_cast(_Float16, (u16)(ur[1] & 0xffffu)) * (1.0f / 512.0f);
    uu[3] = (float)__builtin_bit_cast(_Float16, (u16)(ur[1] >> 16)) * (1.0f / 512.0f);
    const u32x2 xo = T ? xo1 : xo0;
    float xx[4] = {lo16(xo[0]), hi16(xo[0]), lo16(xo[1]), hi16(xo[1])};
    float r[4];
#pragma unroll
    for (int e = 0; e < 4; e++) {
      float z = rbf(rbf(y[e] * kinv) + rbf(uu[e] * dsk));
      r[e] = xx[e] + z;
    }
    u32x2 o = {pk2(r[0], r[1]), pk2(r[2], r[3])};
    if (valid) *(u32x2*)(outT + (size_t)d * L_ + l) = o;
  }
  STAMP(ws, 1, 4);
}

template <int MODE>
__global__ __launch_bounds__(MODE == 0 ? 704 : 640) void k_gemm(const u16* __restrict__ XT, const u32x4* __restrict__ Wp,
                                                                const u16* __restrict__ resT, u16* __restrict__ out,
                                                                char* __restrict__ ws, f32x2* __restrict__ stout) {
  constexpr int NT = (MODE == 0) ? 11 : 10;
  constexpr int NTH = NT * 64;
  __shared__ __attribute__((aligned(16))) char smem[40960 + 32 * 164 * 4];
  const int t = threadIdx.x, lane = t & 63, w = __builtin_amdgcn_readfirstlane(t >> 6);
  const int b = blockIdx.x;
  const int cg = (b >> 3) & 3, rb = (b & 7) + 8 * (b >> 5);
  const int l0 = rb * 32;
  const int g = lane >> 4, li = lane & 15;
  const int ntile = cg * NT + w;
  STAMP(ws, 2 + MODE, 0);
  u32x4 xv[4];
#pragma unroll
  for (int i = 0; i < 4; i++) {
    const int c = t + NTH * i;
    if (c < 2560) xv[i] = *(const u32x4*)(XT + (size_t)(c >> 2) * L_ + l0 + (c & 3) * 8);
  }
  const u32x4* wp0 = Wp + (size_t)ntile * 20 * 64 + lane;
  u32x4 wf[20];
#pragma unroll
  for (int p = 0; p < 3; p++) wf[p] = wp0[p * 64];
  __builtin_amdgcn_sched_barrier(0);
#pragma unroll
  for (int i = 0; i < 4; i++) {
    const int c = t + NTH * i;
    const int row = c >> 2;
    if (c < 2560) *(u32x4*)(smem + row * 64 + (((c & 3) ^ (2 * ((row >> 3) & 1))) * 16)) = xv[i];
  }
  __syncthreads();
  STAMP(ws, 2 + MODE, 1);
  f32x4 acc[2] = {{0, 0, 0, 0}, {0, 0, 0, 0}};
  const int q4 = li >> 2, p4 = li & 3;
  const unsigned sbase = (unsigned)(size_t)smem;
  unsigned xa[2];
#pragma unroll
  for (int lt = 0; lt < 2; lt++)
    xa[lt] = sbase + (8 * g + q4) * 64 + (((2 * lt + (p4 >> 1)) ^ (2 * (g & 1))) * 16) + 8 * (p4 & 1);
  f32x2* stp = (f32x2*)(smem + 40960);
  if (MODE == 0) {
#pragma unroll
    for (int p = 3; p < 9; p++) wf[p] = wp0[p * 64];
    __builtin_amdgcn_sched_barrier(0);
    float ssum[2] = {0.f, 0.f}, ssq[2] = {0.f, 0.f};
    const bf16x2 ones = __builtin_bit_cast(bf16x2, 0x3F803F80u);
#pragma unroll
    for (int kk = 0; kk < 2; kk++) {
      const int ks = w + NT * kk;
      if (ks < 20) {
#pragma unroll
        for (int lt = 0; lt < 2; lt++) {
          const u32x4 xf = tr_frag(xa[lt] + ks * 2048, xa[lt] + ks * 2048 + 256);
#pragma unroll
          for (int e = 0; e < 4; e++) {
            const unsigned ux = xf[e];
            bf16x2 xx = __builtin_bit_cast(bf16x2, ux);
            ssum[lt] = __builtin_amdgcn_fdot2_f32_bf16(xx, ones, ssum[lt], false);
            ssq[lt] = __builtin_amdgcn_fdot2_f32_bf16(xx, xx, ssq[lt], false);
          }
        }
      }
    }
#pragma unroll
    for (int lt = 0; lt < 2; lt++) {
      ssum[lt] += __shfl_xor(ssum[lt], 16); ssum[lt] += __shfl_xor(ssum[lt], 32);
      ssq[lt] += __shfl_xor(ssq[lt], 16); ssq[lt] += __shfl_xor(ssq[lt], 32);
      if (g == 0) { f32x2 o = {ssum[lt], ssq[lt]}; stp[w * 32 + 16 * lt + li] = o; }
    }
  }
  __builtin_amdgcn_sched_barrier(0);
#pragma unroll
  for (int p = (MODE == 0) ? 9 : 3; p < 20; p++) wf[p] = wp0[p * 64];
  f32x4 c4;
  u32x2 xr[2];
  u16 xs[2][4];
  if (MODE == 0) c4 = *(const f32x4*)((const float*)(ws + OFF_CS) + ntile * 16 + 4 * g);
  if (MODE == 1) {
#pragma unroll
    for (int lt = 0; lt < 2; lt++) xr[lt] = *(const u32x2*)(resT + (size_t)(ntile * 16 + li) * L_ + l0 + 16 * lt + 4 * g);
  }
  if (MODE == 2) {
#pragma unroll
    for (int lt = 0; lt < 2; lt++)
#pragma unroll
      for (int e = 0; e < 4; e++) xs[lt][e] = resT[(size_t)(ntile * 16 + 4 * g + e) * L_ + l0 + 16 * lt + li];
  }
#pragma unroll
  for (int ks = 0; ks < 20; ks++) {
    u32x4 xf[2];
#pragma unroll
    for (int lt = 0; lt < 2; lt++) xf[lt] = tr_frag(xa[lt] + ks * 2048, xa[lt] + ks * 2048 + 256);
    const bf16x8 wv = __builtin_bit_cast(bf16x8, wf[ks]);
#pragma unroll
    for (int lt = 0; lt < 2; lt++) {
      if (MODE == 1) acc[lt] = __builtin_amdgcn_mfma_f32_16x16x32_bf16(__builtin_bit_cast(bf16x8, xf[lt]), wv, acc[lt], 0, 0, 0);
      else acc[lt] = __builtin_amdgcn_mfma_f32_16x16x32_bf16(wv, __builtin_bit_cast(bf16x8, xf[lt]), acc[lt], 0, 0, 0);
    }
  }
  STAMP(ws, 2 + MODE, 2);
  if (MODE == 0) {
    __syncthreads();
    f32x2 svv[2];
#pragma unroll
    for (int lt = 0; lt < 2; lt++) {
      f32x2 a = {0.f, 0.f};
#pragma unroll
      for (int i = 0; i < 11; i++) a += stp[i * 32 + 16 * lt + li];
      float mean = a[0] * (1.0f / 640.0f);
      float var = fmaxf(a[1] * (1.0f / 640.0f) - mean * mean, 0.f);
      float sd = rbf(__builtin_amdgcn_sqrtf(rbf(rbf(var) + 1e-5f)));
      svv[lt] = (f32x2){rbf(mean), __builtin_amdgcn_rcpf(sd)};
    }
    u16* qo = (u16*)(ws + OFF_Q);
    char* kvimg = ws + OFF_KV;
    const int n0 = ntile * 16 + 4 * g;
#pragma unroll
    for (int lt = 0; lt < 2; lt++) {
      const f32x2 sv = svv[lt];
      const int l = l0 + 16 * lt + li;
      float v[4];
#pragma unroll
      for (int r = 0; r < 4; r++) v[r] = (acc[lt][r] - sv[0] * c4[r]) * sv[1];
      if (n0 < 640) {
        u32x2 o = {pk2(v[0] * QSCALE, v[1] * QSCALE), pk2(v[2] * QSCALE, v[3] * QSCALE)};
        *(u32x2*)(qo + (size_t)l * D_ + n0) = o;
      } else if (n0 < 660) {
        const int dh = n0 - 640;
        u32x2 o = {pk2(v[0], v[1]), pk2(v[2], v[3])};
        const int tile = l >> 5, ln = (l & 31) + 32 * ((dh & 15) >> 3);
        if (dh < 16) *(u32x2*)(kvimg + tile * KV_TB + ln * 16 + (dh & 7) * 2) = o;
        else *(u32x2*)(kvimg + tile * KV_TB + KV_K1 + (l & 31) * 8) = o;
      } else if (n0 < 680) {
        const int dv = n0 - 660;
        const int tile = l >> 5, kk = l & 15, step = (l & 31) >> 4;
        const int hh = (kk & 7) >> 2, jj = (kk >> 3) * 4 + (kk & 3);
#pragma unroll
        for (int r = 0; r < 4; r++)
          *(u16*)(kvimg + tile * KV_TB + KV_V0 + step * 672 + hh * 336 + (dv + r) * 16 + jj * 2) = f2bf(v[r]);
      }
    }
  } else if (MODE == 1) {
    float* stg = (float*)(smem + 40960);
    const int nl = w * 16 + li;
    const int n = cg * 160 + nl;
#pragma unroll
    for (int lt = 0; lt < 2; lt++) {
      const int ll = 16 * lt + 4 * g;
      float r[4];
      r[0] = rbf(lo16(xr[lt][0]) + rbf(acc[lt][0]));
      r[1] = rbf(hi16(xr[lt][0]) + rbf(acc[lt][1]));
      r[2] = rbf(lo16(xr[lt][1]) + rbf(acc[lt][2]));
      r[3] = rbf(hi16(xr[lt][1]) + rbf(acc[lt][3]));
      u32x2 o = {pk2(r[0], r[1]), pk2(r[2], r[3])};
      *(u32x2*)(out + (size_t)n * L_ + l0 + ll) = o;
#pragma unroll
      for (int e = 0; e < 4; e++) stg[(ll + e) * 164 + nl] = r[e];
    }
    __syncthreads();
    if (t < 256) {
      const int row = t >> 3, part = t & 7;
      float v[20];
      float s = 0.f;
#pragma unroll
      for (int i = 0; i < 5; i++) {
        f32x4 a = *(const f32x4*)(stg + row * 164 + part * 20 + 4 * i);
#pragma unroll
        for (int e = 0; e < 4; e++) { v[4 * i + e] = a[e]; s += a[e]; }
      }
      s += __shfl_xor(s, 1); s += __shfl_xor(s, 2); s += __shfl_xor(s, 4);
      const float mean = s * (1.0f / 160.0f);
      float m2 = 0.f;
#pragma unroll
      for (int i = 0; i < 20; i++) { float a = v[i] - mean; m2 += a * a; }
      m2 += __shfl_xor(m2, 1); m2 += __shfl_xor(m2, 2); m2 += __shfl_xor(m2, 4);
      if (part == 0) { f32x2 o = {mean, m2}; stout[cg * L_ + l0 + row] = o; }
    }
  } else {
    const int n0 = ntile * 16 + 4 * g;
#pragma unroll
    for (int lt = 0; lt < 2; lt++) {
      const int l = l0 + 16 * lt + li;
      float r[4];
#pragma unroll
      for (int e = 0; e < 4; e++) r[e] = bf2f(xs[lt][e]) + rbf(acc[lt][e]);
      u32x2 o = {pk2(r[0], r[1]), pk2(r[2], r[3])};
      *(u32x2*)(out + (size_t)l * D_ + n0) = o;
    }
  }
  STAMP(ws, 2 + MODE, 3);
}

__device__ __forceinline__ void pack_p(const f32x16& s, bf16x8& p0, bf16x8& p1) {
  u32x4 a, c;
#pragma unroll
  for (int i = 0; i < 4; i++) {
    a[i] = pk2(__builtin_amdgcn_exp2f(s[2 * i]), __builtin_amdgcn_exp2f(s[2 * i + 1]));
    c[i] = pk2(__builtin_amdgcn_exp2f(s[8 + 2 * i]), __builtin_amdgcn_exp2f(s[9 + 2 * i]));
  }
  p0 = __builtin_bit_cast(bf16x8, a);
  p1 = __builtin_bit_cast(bf16x8, c);
}
__device__ __forceinline__ float max16(const f32x16& s) {
  float m = fmaxf(s[0], s[1]);
#pragma unroll
  for (int i = 2; i < 16; i++) m = fmaxf(m, s[i]);
  return m;
}

__device__ __forceinline__ u32x4 k1frag(u32x2 v) { return (u32x4){v[0], v[1], 0u, 0u}; }

__global__ __launch_bounds__(1024) void k_attn(const char* __restrict__ ws_q, const char* __restrict__ kv,
                                               u16* __restrict__ oT) {
  __shared__ __attribute__((aligned(16))) char sm[62 * KV_TB];
  const int t = threadIdx.x, lane = t & 63, w = __builtin_amdgcn_readfirstlane(t >> 6);
  const int b = blockIdx.x;
  const int kh = w >> 3;
  const int pb = (b & 7) + 8 * (b >> 5), head = ((b >> 3) & 3) * 8 + (w & 7);
  const int ci = lane & 31, h = lane >> 5;
  const int l = pb * 32 + ci;
  STAMP(ws_q - OFF_Q, 5, 0);
#pragma unroll
  for (int hh = 0; hh < 2; hh++)
    for (int i = w * 64; i < 8 * 164; i += 1024)
      if (i + lane < 8 * 164)
        __builtin_amdgcn_global_load_lds((const __attribute__((address_space(1))) void*)(kv + (size_t)hh * 32 * KV_TB + (i + lane) * 16),
                                         (LDS_AS void*)(sm + hh * 31 * KV_TB + (i + lane) * 16), 16, 0, 0);
  bf16x8 q0, q1;
  s16x4 q1s;
  {
    const char* qp = ws_q + (size_t)l * (D_ * 2) + head * 40;
    u32x2 a = *(const u32x2*)(qp + 16 * h), c = *(const u32x2*)(qp + 16 * h + 8);
    u32x2 e = *(const u32x2*)(qp + 32);
    u32x4 f0 = {a[0], a[1], c[0], c[1]};
    u32x4 f1 = {h ? 0u : e[0], h ? 0u : e[1], 0u, 0u};
    q0 = __builtin_bit_cast(bf16x8, f0);
    q1 = __builtin_bit_cast(bf16x8, f1);
    u32x2 f1s = {f1[0], f1[1]};
    q1s = __builtin_bit_cast(s16x4, f1s);
  }
  __syncthreads();
#define DMA_B(j)                                                                                                      \
  {                                                                                                                   \
    const int hh_ = (j) & 1, i_ = 8 * 164 + w * 64 + ((j) >> 1) * 1024;                                               \
    if (i_ + lane < 31 * 164)                                                                                         \
      __builtin_amdgcn_global_load_lds((const __attribute__((address_space(1))) void*)(kv + (size_t)hh_ * 32 * KV_TB + (i_ + lane) * 16), \
                                       (LDS_AS void*)(sm + hh_ * 31 * KV_TB + (i_ + lane) * 16), 16, 0, 0);          \
  }
  DMA_B(0) DMA_B(1)
  STAMP(ws_q - OFF_Q, 5, 1);
  STAMPC(ws_q - OFF_Q, 6, 0);
  const int ok0 = lane * 16, ok1 = KV_K1 + ci * 8, ov0 = KV_V0 + h * 336 + min(ci, 20) * 16, ov1 = ov0 + 672;
  f32x16 negm;
  {
    f32x16 z = {0};
    f32x16 s = __builtin_amdgcn_mfma_f32_32x32x16_bf16(*(const bf16x8*)(sm + ok0), q0, z, 0, 0, 0);
    s = __builtin_amdgcn_mfma_f32_32x32x16_bf16(__builtin_bit_cast(bf16x8, k1frag(*(const u32x2*)(sm + ok1))), q1, s, 0, 0, 0);
    float m = max16(s);
    m = fmaxf(m, __shfl_xor(m, 32));
#pragma unroll
    for (int i = 0; i < 16; i++) negm[i] = -m;
  }
  f32x16 o = {0};
#define SB() __builtin_amdgcn_sched_barrier(0)
  const char* base = sm + kh * 31 * KV_TB;
  const char* gl = kv + (size_t)(kh * 32 + 31) * KV_TB;
  u32x4 k0n = *(const u32x4*)(base + ok0);
  u32x2 k1n = *(const u32x2*)(base + ok1);
  u32x4 v0 = *(const u32x4*)(base + ov0), v1 = *(const u32x4*)(base + ov1);
  f32x16 s = __builtin_amdgcn_mfma_f32_32x32x16_bf16(__builtin_bit_cast(bf16x8, k0n), q0, negm, 0, 0, 0);
  s = __builtin_amdgcn_mfma_f32_32x32x16_bf16(__builtin_bit_cast(bf16x8, k1frag(k1n)), q1, s, 0, 0, 0);
  k0n = *(const u32x4*)(base + KV_TB + ok0);
  k1n = *(const u32x2*)(base + KV_TB + ok1);
#define TILE(KP, VP, NEXT)                                                                                            \
  {                                                                                                                   \
    u32x4 pa, pb;                                                                                                     \
    f32x16 sn;                                                                                                        \
    if (NEXT) sn = __builtin_amdgcn_mfma_f32_32x32x16_bf16(__builtin_bit_cast(bf16x8, k0n), q0, negm, 0, 0, 0);       \
    SB();                                                                                                             \
    pa[0] = pk2(__builtin_amdgcn_exp2f(s[0]), __builtin_amdgcn_exp2f(s[1]));                                          \
    pa[1] = pk2(__builtin_amdgcn_exp2f(s[2]), __builtin_amdgcn_exp2f(s[3]));                                          \
    SB();                                                                                                             \
    if (NEXT) sn = __builtin_amdgcn_mfma_f32_32x32x8bf16_1k(__builtin_bit_cast(s16x4, k1n), q1s, sn, 0, 0, 0);       \
    u32x4 v0n = v0, v1n = v1;                                                                                         \
    if (NEXT) { k0n = *(const u32x4*)((KP) + ok0); k1n = *(const u32x2*)((KP) + ok1); }                               \
    if (NEXT) { v0n = *(const u32x4*)((VP) + ov0); v1n = *(const u32x4*)((VP) + ov1); }                               \
    SB();                                                                                                             \
    pa[2] = pk2(__builtin_amdgcn_exp2f(s[4]), __builtin_amdgcn_exp2f(s[5]));                                          \
    pa[3] = pk2(__builtin_amdgcn_exp2f(s[6]), __builtin_amdgcn_exp2f(s[7]));                                          \
    pb[0] = pk2(__builtin_amdgcn_exp2f(s[8]), __builtin_amdgcn_exp2f(s[9]));                                          \
    SB();                                                                                                             \
    o = __builtin_amdgcn_mfma_f32_32x32x16_bf16(__builtin_bit_cast(bf16x8, v0), __builtin_bit_cast(bf16x8, pa), o, 0, 0, 0); \
    SB();                                                                                                             \
    pb[1] = pk2(__builtin_amdgcn_exp2f(s[10]), __builtin_amdgcn_exp2f(s[11]));                                        \
    pb[2] = pk2(__builtin_amdgcn_exp2f(s[12]), __builtin_amdgcn_exp2f(s[13]));                                        \
    pb[3] = pk2(__builtin_amdgcn_exp2f(s[14]), __builtin_amdgcn_exp2f(s[15]));                                        \
    SB();                                                                                                             \
    o = __builtin_amdgcn_mfma_f32_32x32x16_bf16(__builtin_bit_cast(bf16x8, v1), __builtin_bit_cast(bf16x8, pb), o, 0, 0, 0); \
    v0 = v0n; v1 = v1n;                                                                                               \
    if (NEXT) s = sn;                                                                                                 \
    SB();                                                                                                             \
  }
  __builtin_amdgcn_s_setprio(3);
#pragma unroll
  for (int tl = 0; tl < 6; tl++) {
    if (tl < 3) { DMA_B(2 * tl + 2) DMA_B(2 * tl + 3) }
    TILE(base + (tl + 2) * KV_TB, base + (tl + 1) * KV_TB, true)
  }
  __syncthreads();
#pragma unroll 2
  for (int tl = 6; tl < 20; tl++) TILE(base + (tl + 2) * KV_TB, base + (tl + 1) * KV_TB, true)
  __builtin_amdgcn_s_setprio(2);
#pragma unroll 2
  for (int tl = 20; tl < 28; tl++) TILE(base + (tl + 2) * KV_TB, base + (tl + 1) * KV_TB, true)
  __builtin_amdgcn_s_setprio(1);
  TILE(base + 30 * KV_TB, base + 29 * KV_TB, true)
  TILE(gl, base + 30 * KV_TB, true)
  TILE(gl, gl, true)
  __builtin_amdgcn_s_setprio(0);
  TILE(gl, gl, false)
  STAMP(ws_q - OFF_Q, 5, 2);
  STAMPC(ws_q - OFF_Q, 6, 1);
  __syncthreads();
  f32x4* mg = (f32x4*)sm;
  if (kh == 1) {
#pragma unroll
    for (int i = 0; i < 3; i++) {
      f32x4 a = {o[4 * i], o[4 * i + 1], o[4 * i + 2], o[4 * i + 3]};
      mg[((w & 7) * 3 + i) * 64 + lane] = a;
    }
  }
  __syncthreads();
  if (kh == 1) return;
#pragma unroll
  for (int i = 0; i < 3; i++) {
    f32x4 a = mg[(w * 3 + i) * 64 + lane];
#pragma unroll
    for (int e = 0; e < 4; e++) o[4 * i + e] += a[e];
  }
  float lsum = __shfl(o[8], ci + 32);
  bool bad = !(lsum <= 1.8446744e19f);
#pragma unroll
  for (int i = 0; i < 12; i++) bad = bad || !(fabsf(o[i]) <= 3.0e38f);
  if (__any(bad)) {
    float m = -INFINITY;
#pragma unroll
    for (int i = 0; i < 16; i++) o[i] = 0.f;
    for (int tl = 0; tl < 64; tl++) {
      const char* tp = kv + (size_t)tl * KV_TB;
      f32x16 z = {0};
      f32x16 s2 = __builtin_amdgcn_mfma_f32_32x32x16_bf16(*(const bf16x8*)(tp + ok0), q0, z, 0, 0, 0);
      s2 = __builtin_amdgcn_mfma_f32_32x32x16_bf16(__builtin_bit_cast(bf16x8, k1frag(*(const u32x2*)(tp + ok1))), q1, s2, 0, 0, 0);
      float tm = max16(s2);
      tm = fmaxf(tm, __shfl_xor(tm, 32));
      const float mn = fmaxf(m, tm);
      const float al = __builtin_amdgcn_exp2f(m - mn);
      m = mn;
#pragma unroll
      for (int i = 0; i < 16; i++) { o[i] *= al; s2[i] -= mn; }
      bf16x8 p0, p1;
      pack_p(s2, p0, p1);
      o = __builtin_amdgcn_mfma_f32_32x32x16_bf16(*(const bf16x8*)(tp + ov0), p0, o, 0, 0, 0);
      o = __builtin_amdgcn_mfma_f32_32x32x16_bf16(*(const bf16x8*)(tp + ov1), p1, o, 0, 0, 0);
    }
    lsum = __shfl(o[8], ci + 32);
  }
  const float inv = 1.0f / lsum;
  u16* op = oT + (size_t)(head * 20 + 4 * h) * L_ + l;
#pragma unroll
  for (int i = 0; i < 12; i++) {
    const int dd = (i & 3) + 8 * (i >> 2);
    if (i < 8 || h == 0) op[(size_t)dd * L_] = f2bf(o[i] * inv);
  }
  STAMP(ws_q - OFF_Q, 5, 3);
}

extern "C" void kernel_launch(void* const* d_in, const int* in_sizes, int n_in, void* d_out, int out_size,
                              void* d_ws, size_t ws_size, hipStream_t stream) {
  const u16* x = (const u16*)d_in[0];
  const u16* logA = (const u16*)d_in[1];
  const u16* Aim = (const u16*)d_in[2];
  const u16* Cre = (const u16*)d_in[3];
  const u16* Cim = (const u16*)d_in[4];
  const u16* logdt = (const u16*)d_in[5];
  const u16* dskip = (const u16*)d_in[6];
  const u16* wq = (const u16*)d_in[8];
  const u16* wk = (const u16*)d_in[9];
  const u16* wv = (const u16*)d_in[10];
  const u16* wo = (const u16*)d_in[11];
  char* ws = (char*)d_ws;
  u16* xt0 = (u16*)(ws + OFF_XT0);
  u16* x1t = (u16*)(ws + OFF_X1T);
  u16* x2t = (u16*)(ws + OFF_X2T);
  u16* x3t = (u16*)(ws + OFF_X3T);
  u16* ot = (u16*)(ws + OFF_OT);
  const u32x4* wqkv = (const u32x4*)(ws + OFF_WQKV);
  const u32x4* wop = (const u32x4*)(ws + OFF_WO);
  const char* kv = ws + OFF_KV;
  f32x2* st0 = (f32x2*)(ws + OFF_ST0);
  f32x2* st1 = (f32x2*)(ws + OFF_ST1);

  k_prep<<<PREP_WGS, 1024, 0, stream>>>(x, logA, Aim, Cre, Cim, logdt, wq, wk, wv, wo, ws);
  k_conv<true><<<214, 768, 0, stream>>>(xt0, st0, ws, dskip, x1t);
  k_gemm<0><<<256, 704, 0, stream>>>(x1t, wqkv, nullptr, nullptr, ws, nullptr);
  k_attn<<<256, 1024, 0, stream>>>(ws + OFF_Q, kv, ot);
  k_gemm<1><<<256, 640, 0, stream>>>(ot, wop, x1t, x2t, ws, st1);
  k_conv<false><<<214, 768, 0, stream>>>(x2t, st1, ws, dskip, x3t);
  k_gemm<0><<<256, 704, 0, stream>>>(x3t, wqkv, nullptr, nullptr, ws, nullptr);
  k_attn<<<256, 1024, 0, stream>>>(ws + OFF_Q, kv, ot);
  k_gemm<2><<<256, 640, 0, stream>>>(ot, wop, x3t, (u16*)d_out, ws, nullptr);
}
```

```cpp
#include <hip/hip_runtime.h>
#include <hip/hip_bf16.h>
#include <stdint.h>

typedef unsigned short u16;
typedef __attribute__((ext_vector_type(8))) __bf16 bf16x8;
typedef __attribute__((ext_vector_type(8))) _Float16 f16x8;
typedef __attribute__((ext_vector_type(16))) float f32x16;
typedef __attribute__((ext_vector_type(4))) float f32x4;
typedef __attribute__((ext_vector_type(4))) unsigned u32x4;
typedef __attribute__((ext_vector_type(2))) unsigned u32x2;
typedef __attribute__((ext_vector_type(4))) short s16x4;
typedef __attribute__((ext_vector_type(2))) float f32x2;

#define LDS_AS __attribute__((address_space(3)))

constexpr int L_ = 2048, D_ = 640, NS_ = 128;
constexpr int KFROW = 2304;
constexpr int KV_TB = 2624, KV_K1 = 1024, KV_V0 = 1280, KV_V1 = 1952;
constexpr float LOG2E = 1.4426950408889634f;
constexpr float INV2PI = 0.15915494309189535f;
constexpr float QSCALE = 0.22360679774997896f * 1.4426950408889634f;

constexpr size_t SZ_ACT = (size_t)D_ * L_ * 2;
constexpr size_t OFF_XT0 = 0;
constexpr size_t OFF_X1T = OFF_XT0 + SZ_ACT;
constexpr size_t OFF_X2T = OFF_X1T + SZ_ACT;
constexpr size_t OFF_X3T = OFF_X2T + SZ_ACT;
constexpr size_t OFF_Q = OFF_X3T + SZ_ACT;
constexpr size_t OFF_OT = OFF_Q + SZ_ACT;
constexpr size_t OFF_KF = OFF_OT + SZ_ACT;
constexpr size_t OFF_KINV = OFF_KF + (size_t)D_ * KFROW * 2;
constexpr size_t OFF_WQKV = OFF_KINV + 4096;
constexpr size_t OFF_WO = OFF_WQKV + (size_t)44 * 20 * 1024;
constexpr size_t OFF_CS = OFF_WO + (size_t)40 * 20 * 1024;
constexpr size_t OFF_KV = OFF_CS + 4096;
constexpr size_t OFF_ST0 = OFF_KV + 262144;
constexpr size_t OFF_ST1 = OFF_ST0 + 65536;
constexpr size_t OFF_CNT = OFF_ST1 + 65536;

__device__ __forceinline__ float bf2f(u16 v) { return __uint_as_float(((unsigned)v) << 16); }
__device__ __forceinline__ u16 f2bf(float f) { __bf16 b = (__bf16)f; return __builtin_bit_cast(u16, b); }
__device__ __forceinline__ float rbf(float f) { return bf2f(f2bf(f)); }
typedef __attribute__((ext_vector_type(2))) __bf16 bf16x2;
__device__ __forceinline__ unsigned pk2(float a, float b) {
  f32x2 v = {a, b};
  return __builtin_bit_cast(unsigned, __builtin_convertvector(v, bf16x2));
}
__device__ __forceinline__ float lo16(unsigned v) { return __uint_as_float(v << 16); }
__device__ __forceinline__ float hi16(unsigned v) { return __uint_as_float(v & 0xffff0000u); }

constexpr int PREP_WGS = 256;
constexpr int PREP_UNIT_LDS = 23808;

__device__ __forceinline__ int hi_value(int idx) {
  int v;
  if (idx < 16) v = 128 * (idx >> 3) + (idx & 7);
  else if (idx < 24) v = 256 + 128 * ((idx - 16) >> 2) + 2 * ((idx - 16) & 3);
  else if (idx < 32) v = 512 + 128 * ((idx - 24) >> 1) + 4 * ((idx - 24) & 1);
  else if (idx <= 40) v = 1024 + 128 * (idx - 32);
  else v = 0;
  return v;
}

__device__ __forceinline__ u32x4 tr_frag(unsigned addr0, unsigned addr1) {
  s16x4 a = __builtin_amdgcn_ds_read_tr16_b64_v4i16((LDS_AS s16x4*)(size_t)addr0);
  s16x4 c = __builtin_amdgcn_ds_read_tr16_b64_v4i16((LDS_AS s16x4*)(size_t)addr1);
  u32x2 a2 = __builtin_bit_cast(u32x2, a), c2 = __builtin_bit_cast(u32x2, c);
  return (u32x4){a2[0], a2[1], c2[0], c2[1]};
}
__device__ __forceinline__ void split2(float a, float b, unsigned& hi, unsigned& lo) {
  hi = __builtin_bit_cast(unsigned, __builtin_amdgcn_cvt_pkrtz(a, b));
  float la, lb;
  asm("v_fma_mix_f32 %0, %1, -1.0, %2 op_sel_hi:[1,0,0]" : "=v"(la) : "v"(hi), "v"(a));
  asm("v_fma_mix_f32 %0, %1, -1.0, %2 op_sel:[1,0,0] op_sel_hi:[1,0,0]" : "=v"(lb) : "v"(hi), "v"(b));
  lo = __builtin_bit_cast(unsigned, __builtin_amdgcn_cvt_pkrtz(la, lb));
}

__global__ __launch_bounds__(1024) void k_prep(const u16* __restrict__ x, const u16* __restrict__ logA,
                                               const u16* __restrict__ Aim, const u16* __restrict__ Cre,
                                               const u16* __restrict__ Cim, const u16* __restrict__ logdt,
                                               const u16* __restrict__ wq, const u16* __restrict__ wk,
                                               const u16* __restrict__ wv, const u16* __restrict__ wo,
                                               char* __restrict__ ws) {
  __shared__ __attribute__((aligned(16))) char smem_all[4 * PREP_UNIT_LDS];
  const int ug = __builtin_amdgcn_readfirstlane(threadIdx.x >> 8);
  char* smem = smem_all + ug * PREP_UNIT_LDS;
  const int t = threadIdx.x & 255, lane = t & 63, w = __builtin_amdgcn_readfirstlane(t >> 6);
  const unsigned sbase = (unsigned)(size_t)smem;
  const int g = lane >> 4, li = lane & 15, q4 = li >> 2, p4 = li & 3;
  const int b = blockIdx.x;
  if (ug < 3 && b >= 214) {
    __syncthreads(); __syncthreads(); __syncthreads();
  } else if (ug < 3) {
    const int d = min(b * 3 + ug, D_ - 1);
    float* prm = (float*)smem;
    float* part = prm + 512;
    float* tab = part + 4 * 768;
    float* red = tab + 2312;
    if (t < 128) {
      const float dt = rbf(expf(bf2f(logdt[d])));
      float ar = -rbf(expf(bf2f(logA[d * NS_ + t])));
      float alpha = ar * dt, beta = bf2f(Aim[d * NS_ + t]) * dt;
      const float cr = 2.0f * (bf2f(Cre[d * NS_ + t]) * dt), ci = 2.0f * (bf2f(Cim[d * NS_ + t]) * dt);
      float bnd = hypotf(cr, ci);
      f32x4 pv = {alpha * LOG2E, beta * INV2PI, log2f(bnd), atan2f(ci, cr) * INV2PI};
      *(f32x4*)(prm + t * 4) = pv;
#pragma unroll
      for (int o = 32; o > 0; o >>= 1) bnd += __shfl_xor(bnd, o);
      if (lane == 0) red[w] = bnd;
    }
    __syncthreads();
    const float bnd = red[0] + red[1];
    int ex = (int)((__float_as_uint(bnd) >> 23) & 0xff) - 126;
    if (!(bnd > 1e-30f && bnd < 1e30f)) ex = 15;
    const float sA = (float)(14 - ex);
    const float lo = 8.0f * li;
    float hv[3] = {(float)hi_value(li), (float)hi_value(li + 16), (float)hi_value(li + 32)};
    f32x4 acc[3] = {{0, 0, 0, 0}, {0, 0, 0, 0}, {0, 0, 0, 0}};
#pragma unroll 1
    for (int ks = 0; ks < 2; ks++) {
      u32x4 bh, bl, ah[3], al[3];
#pragma unroll
      for (int m = 0; m < 4; m++) {
        const int n = 32 * w + 16 * ks + 4 * g + m;
        f32x4 p = *(const f32x4*)(prm + n * 4);
        p[2] += sA;
        {
          float e = __builtin_amdgcn_exp2f(fmaf(p[0], lo, 14.0f));
          float ph = __builtin_amdgcn_fractf(p[1] * lo);
          unsigned h_, l_;
          split2(e * __builtin_amdgcn_cosf(ph), e * __builtin_amdgcn_sinf(ph), h_, l_);
          bh[m] = h_; bl[m] = l_;
        }
#pragma unroll
        for (int tt = 0; tt < 3; tt++) {
          float e2 = __builtin_amdgcn_exp2f(fmaf(p[0], hv[tt], p[2]));
          float ph2 = __builtin_amdgcn_fractf(fmaf(p[1], hv[tt], p[3]));
          unsigned h_, l_;
          split2(e2 * __builtin_amdgcn_cosf(ph2), -e2 * __builtin_amdgcn_sinf(ph2), h_, l_);
          ah[tt][m] = h_; al[tt][m] = l_;
        }
      }
#pragma unroll
      for (int tt = 0; tt < 3; tt++) {
        acc[tt] = __builtin_amdgcn_mfma_f32_16x16x32_f16(__builtin_bit_cast(f16x8, al[tt]), __builtin_bit_cast(f16x8, bh), acc[tt], 0, 0, 0);
        acc[tt] = __builtin_amdgcn_mfma_f32_16x16x32_f16(__builtin_bit_cast(f16x8, ah[tt]), __builtin_bit_cast(f16x8, bl), acc[tt], 0, 0, 0);
        acc[tt] = __builtin_amdgcn_mfma_f32_16x16x32_f16(__builtin_bit_cast(f16x8, ah[tt]), __builtin_bit_cast(f16x8, bh), acc[tt], 0, 0, 0);
      }
    }
#pragma unroll
    for (int tt = 0; tt < 3; tt++)
#pragma unroll
      for (int r = 0; r < 4; r++) part[w * 768 + (16 * tt + g * 4 + r) * 16 + li] = acc[tt][r];
    __syncthreads();
#pragma unroll
    for (int k = 0; k < 3; k++) {
      const int i = t + 256 * k;
      const int hi = i >> 4, lo_i = i & 15;
      if (hi < 40 || (hi == 40 && lo_i == 0))
      {
        const int pp = hi_value(hi) + 8 * lo_i;
        tab[pp + (pp >> 3)] = (part[i] + part[768 + i]) + (part[1536 + i] + part[2304 + i]);
      }
    }
    __syncthreads();
    const float sc = 1.0f / 8192.0f;
    f16x8 o;
#pragma unroll
    for (int e = 0; e < 8; e++) {
      const int pp = (int)rbf((float)(8 * t + e));
      o[e] = (_Float16)(tab[pp + (pp >> 3)] * sc);
    }
    f16x8* dst = (f16x8*)(ws + OFF_KF + (size_t)d * KFROW * 2);
    dst[31 + t] = o;
    if (t < 32) {
      f16x8 z = {0, 0, 0, 0, 0, 0, 0, 0};
      dst[t < 31 ? t : 287] = z;
    }
    if (t == 0) ((float*)(ws + OFF_KINV))[d] = __uint_as_float((unsigned)(127 + ex - 15 - 9) << 23);
  } else if (b < 128) {
    __syncthreads();
    const int l0 = b * 16, row = t >> 4, part = t & 15;
    const u32x4* src = (const u32x4*)(x + (size_t)(l0 + row) * D_ + part * 40);
    u32x4 v[5];
#pragma unroll
    for (int i = 0; i < 5; i++) v[i] = src[i];
    if (t == 2) *(unsigned*)(ws + OFF_CNT + b * 256) = 0u;
    if (t < 2) {
      const int c = b * 2 + t;
      const int tile = c >> 2, step = (c >> 1) & 1, hh = c & 1;
      u32x4 o = {0x3F803F80u, 0x3F803F80u, 0x3F803F80u, 0x3F803F80u};
      *(u32x4*)(ws + OFF_KV + tile * KV_TB + KV_V0 + step * 672 + hh * 336 + 320) = o;
    }
    float s = 0.f;
#pragma unroll
    for (int i = 0; i < 5; i++)
#pragma unroll
      for (int j = 0; j < 4; j++) s += lo16(v[i][j]) + hi16(v[i][j]);
#pragma unroll
    for (int o = 1; o < 16; o <<= 1) s += __shfl_xor(s, o);
    const float mean = s * (1.0f / 640.0f);
    float m2 = 0.f;
#pragma unroll
    for (int i = 0; i < 5; i++)
#pragma unroll
      for (int j = 0; j < 4; j++) {
        float a = lo16(v[i][j]) - mean, c = hi16(v[i][j]) - mean;
        m2 += a * a + c * c;
      }
#pragma unroll
    for (int o = 1; o < 16; o <<= 1) m2 += __shfl_xor(m2, o);
    if (part == 0) {
      const float vb = rbf(m2 * (1.0f / 640.0f));
      ((unsigned*)(ws + OFF_ST0))[l0 + row] = pk2(mean, __builtin_amdgcn_sqrtf(rbf(vb + 1e-5f)));
    }
#pragma unroll
    for (int i = 0; i < 5; i++) *(u32x4*)(smem + row * 1296 + part * 80 + i * 16) = v[i];
    __syncthreads();
    u16* xt = (u16*)(ws + OFF_XT0);
#pragma unroll
    for (int i = 0; i < 5; i++) {
      const int blk = (w * 5 + i) * 4 + g;
      const int ct = blk >> 1, rh = blk & 1;
      const unsigned ad = sbase + (rh * 8 + q4) * 1296 + ct * 32 + p4 * 8;
      const u32x4 f = tr_frag(ad, ad + 4 * 1296);
      *(u32x4*)(xt + (size_t)(ct * 16 + li) * L_ + l0 + rh * 8) = f;
    }
    __syncthreads();
  } else if (b < 208) {
    __syncthreads();
    const int u = b - 128;
    const int hf = u & 1, uu = u >> 1;
    const bool isq = uu < 20;
    const int ks = isq ? uu : uu - 20;
    const u16* W = isq ? wq : wo;
    u32x4* dst = (u32x4*)(ws + (isq ? OFF_WQKV : OFF_WO));
    u32x4 v[5];
#pragma unroll
    for (int i = 0; i < 5; i++) {
      const int c = t + 256 * i;
      const int r = c / 40, cc = c - r * 40;
      v[i] = *(const u32x4*)(W + (size_t)(ks * 32 + r) * 640 + hf * 320 + cc * 8);
    }
    const bool dokv = isq && hf == 0;
    u16 e[8];
    const int nkv = (40 + w) * 16 + li;
    if (dokv) {
#pragma unroll
      for (int j = 0; j < 8; j++) {
        const int k = ks * 32 + 8 * g + j;
        e[j] = (nkv < 660) ? wk[k * 20 + (nkv - 640)] : (nkv < 680) ? wv[k * 20 + (nkv - 660)] : (u16)0;
      }
    }
#pragma unroll
    for (int i = 0; i < 5; i++) {
      const int c = t + 256 * i;
      const int r = c / 40, cc = c - r * 40;
      *(u32x4*)(smem + r * 656 + cc * 16) = v[i];
    }
    __syncthreads();
#pragma unroll
    for (int i = 0; i < 5; i++) {
      const int nt = w * 5 + i;
      const unsigned ad = sbase + (8 * g + q4) * 656 + nt * 32 + p4 * 8;
      const u32x4 f = tr_frag(ad, ad + 4 * 656);
      dst[((size_t)(hf * 20 + nt) * 20 + ks) * 64 + lane] = f;
    }
    if (dokv) {
      u32x4 o;
#pragma unroll
      for (int j = 0; j < 4; j++) o[j] = (unsigned)e[2 * j] | ((unsigned)e[2 * j + 1] << 16);
      dst[((size_t)(40 + w) * 20 + ks) * 64 + lane] = o;
    }
    __syncthreads();
  } else if (b < 214) {
    __syncthreads(); __syncthreads(); __syncthreads();
  } else {
    __syncthreads();
    const int u = min(b - 214, 40);
    float sacc[8] = {0, 0, 0, 0, 0, 0, 0, 0};
    float* red = (float*)smem;
    if (u < 40) {
      const int c2 = t & 1, rg = t >> 1;
      const u32x4* src = (const u32x4*)(wq + (size_t)(rg * 5) * 640 + u * 16 + c2 * 8);
      u32x4 v[5];
#pragma unroll
      for (int i = 0; i < 5; i++) v[i] = src[i * 80];
#pragma unroll
      for (int i = 0; i < 5; i++)
#pragma unroll
        for (int j = 0; j < 4; j++) { sacc[2 * j] += lo16(v[i][j]); sacc[2 * j + 1] += hi16(v[i][j]); }
#pragma unroll
      for (int j = 0; j < 8; j++)
#pragma unroll
        for (int o = 2; o < 64; o <<= 1) sacc[j] += __shfl_xor(sacc[j], o);
      if (lane < 2) {
#pragma unroll
        for (int j = 0; j < 8; j++) red[w * 16 + lane * 8 + j] = sacc[j];
      }
    } else {
      const int c10 = t % 10, rg = t / 10;
      if (rg < 25) {
        const u16* src = ((c10 < 5) ? wk + c10 * 4 : wv + (c10 - 5) * 4);
#pragma unroll
        for (int i = 0; i < 26; i++) {
          const int row = rg + 25 * i;
          if (row < 640) {
            const u32x2 v = *(const u32x2*)(src + (size_t)row * 20);
            sacc[0] += lo16(v[0]); sacc[1] += hi16(v[0]); sacc[2] += lo16(v[1]); sacc[3] += hi16(v[1]);
          }
        }
#pragma unroll
        for (int j = 0; j < 4; j++) red[rg * 40 + c10 * 4 + j] = sacc[j];
      }
    }
    __syncthreads();
    if (u < 40) {
      if (t < 16) ((float*)(ws + OFF_CS))[u * 16 + t] = (red[t] + red[16 + t]) + (red[32 + t] + red[48 + t]);
    } else if (t < 64) {
      float v = 0.f;
      if (t < 40) {
#pragma unroll
        for (int i = 0; i < 25; i++) v += red[i * 40 + t];
      }
      ((float*)(ws + OFF_CS))[640 + t] = v;
    }
    __syncthreads();
  }
}

constexpr int CV_COPY = 321;
constexpr int CV_UOFF = 2576;
constexpr int CV_CH = CV_UOFF + 288;
constexpr int CV_ZERO = 3 * CV_CH;
constexpr int CV_STAT = CV_ZERO + 48;
constexpr int CV_TOTAL = CV_STAT + 512;

template <bool FINAL>
__global__ __launch_bounds__(768) void k_conv(const u16* __restrict__ xT, const f32x2* __restrict__ st,
                                              const char* __restrict__ ws, const u16* __restrict__ dskip,
                                              u16* __restrict__ outT) {
  __shared__ __attribute__((aligned(16))) u32x4 sm[CV_TOTAL];
  const int tg = threadIdx.x;
  const int cgp = __builtin_amdgcn_readfirstlane(tg >> 8), t = tg & 255, lane = t & 63;
  const int w = __builtin_amdgcn_readfirstlane(t >> 6);
  const int dreal = blockIdx.x * 3 + cgp;
  const bool valid = dreal < D_;
  const int d = valid ? dreal : D_ - 1;
  const int ci = lane & 31, h = lane >> 5;
  u32x4* cb = sm + cgp * CV_CH;
  const u32x4* kg = (const u32x4*)(ws + OFF_KF + (size_t)d * KFROW * 2);
  const u32x4 kc = kg[31 + t], kp = kg[30 + t];
  f32x2 sp[3][4];
  u32x4 sf0, sf1;
  if (FINAL) {
    sf0 = ((const u32x4*)st)[2 * t];
    sf1 = ((const u32x4*)st)[2 * t + 1];
  } else {
#pragma unroll
    for (int i = 0; i < 3; i++) {
      const int row = min(tg + 768 * i, L_ - 1);
#pragma unroll
      for (int p = 0; p < 4; p++) sp[i][p] = st[p * L_ + row];
    }
  }
  const u32x4 xv = *(const u32x4*)(xT + (size_t)d * L_ + 8 * t);
  const float kinv = ((const float*)(ws + OFF_KINV))[d];
  const int lo0 = 8 * ci + 4 * h + 256 * w;
  const u32x2 xo0 = *(const u32x2*)(xT + (size_t)d * L_ + lo0);
  const u32x2 xo1 = *(const u32x2*)(xT + (size_t)d * L_ + lo0 + 1024);
  const float dsk = bf2f(dskip[d]);
  {
    u32x4 z = {0, 0, 0, 0};
    if (tg < 48) sm[CV_ZERO + tg] = z;
    if (t < 32) cb[CV_UOFF + (t < 31 ? t : 287)] = z;
  }
  {
    unsigned Z[8], Y[7];
#pragma unroll
    for (int m = 0; m < 4; m++) {
      Z[m] = __builtin_amdgcn_alignbit(kc[3 - m], kc[3 - m], 16);
      Z[4 + m] = __builtin_amdgcn_alignbit(kp[3 - m], kp[3 - m], 16);
    }
#pragma unroll
    for (int m = 0; m < 7; m++) Y[m] = __builtin_amdgcn_alignbit(Z[m + 1], Z[m], 16);
    const int phys = (t >> 5) * 40 + (t & 31);
#pragma unroll
    for (int r = 0; r < 8; r++) {
      const int o = 7 - r;
      u32x4 v;
      if ((o & 1) == 0) { v[0] = Z[o / 2]; v[1] = Z[o / 2 + 1]; v[2] = Z[o / 2 + 2]; v[3] = Z[o / 2 + 3]; }
      else { v[0] = Y[o / 2]; v[1] = Y[o / 2 + 1]; v[2] = Y[o / 2 + 2]; v[3] = Y[o / 2 + 3]; }
      cb[r * CV_COPY + phys] = v;
    }
  }
  if (!FINAL) {
#pragma unroll
  for (int i = 0; i < 3; i++) {
    const int row = tg + 768 * i;
    float m0 = sp[i][0][0], m1 = sp[i][1][0], m2 = sp[i][2][0], m3 = sp[i][3][0];
    float mean = 0.25f * ((m0 + m1) + (m2 + m3));
    float a0 = m0 - mean, a1 = m1 - mean, a2 = m2 - mean, a3 = m3 - mean;
    float M2 = ((sp[i][0][1] + sp[i][1][1]) + (sp[i][2][1] + sp[i][3][1])) +
               160.0f * ((a0 * a0 + a1 * a1) + (a2 * a2 + a3 * a3));
    float vb = rbf(M2 * (1.0f / 640.0f));
    float sd = __builtin_amdgcn_sqrtf(rbf(vb + 1e-5f));
    if (row < L_) ((unsigned*)(sm + CV_STAT))[row] = pk2(mean, sd);
  }
  __syncthreads();
  }
  {
    const u32x4 s0 = FINAL ? sf0 : sm[CV_STAT + 2 * t], s1 = FINAL ? sf1 : sm[CV_STAT + 2 * t + 1];
    u32x4 c4;
#pragma unroll
    for (int i = 0; i < 4; i++) {
      const unsigned sa = i < 2 ? s0[2 * i] : s1[2 * i - 4], sb = i < 2 ? s0[2 * i + 1] : s1[2 * i - 3];
      float u0 = rbf(rbf(lo16(xv[i]) - lo16(sa)) * __builtin_amdgcn_rcpf(hi16(sa)));
      float u1 = rbf(rbf(hi16(xv[i]) - lo16(sb)) * __builtin_amdgcn_rcpf(hi16(sb)));
      _Float16 f0 = (_Float16)(u0 * 512.0f), f1 = (_Float16)(u1 * 512.0f);
      c4[i] = (unsigned)__builtin_bit_cast(u16, f0) | ((unsigned)__builtin_bit_cast(u16, f1) << 16);
    }
    cb[CV_UOFF + 31 + t] = c4;
  }
  __syncthreads();
  f32x16 T0 = {0}, T1 = {0};
  {
    const int r = ci & 7, al = ci >> 3;
    const int blo = al - w, bhi = al + 4 - w;
    const int alo = (blo >= 0) ? (cgp * CV_CH + r * CV_COPY + 40 * blo + 1 - h) : (CV_ZERO + r + 8 * (blo & 1) + 1 - h);
    const int ahi = cgp * CV_CH + r * CV_COPY + 40 * bhi + 1 - h;
    const int ua = cgp * CV_CH + CV_UOFF + ci + h + 32 * w;
    u32x4 a_lo = sm[alo + 30], a_hi = sm[ahi + 30], b_a = sm[ua], b_b = sm[ua + 128];
#pragma unroll
    for (int s = 0; s < 16; s++) {
      u32x4 n_lo, n_hi, n_a, n_b;
      if (s < 15) {
        n_lo = sm[alo + 28 - 2 * s]; n_hi = sm[ahi + 28 - 2 * s];
        n_a = sm[ua + 2 * s + 2]; n_b = sm[ua + 2 * s + 130];
      }
      __builtin_amdgcn_sched_barrier(0);
      T0 = __builtin_amdgcn_mfma_f32_32x32x16_f16(__builtin_bit_cast(f16x8, a_lo), __builtin_bit_cast(f16x8, b_a), T0, 0, 0, 0);
      T1 = __builtin_amdgcn_mfma_f32_32x32x16_f16(__builtin_bit_cast(f16x8, a_hi), __builtin_bit_cast(f16x8, b_a), T1, 0, 0, 0);
      T1 = __builtin_amdgcn_mfma_f32_32x32x16_f16(__builtin_bit_cast(f16x8, a_lo), __builtin_bit_cast(f16x8, b_b), T1, 0, 0, 0);
      __builtin_amdgcn_sched_barrier(0);
      if (s < 15) { a_lo = n_lo; a_hi = n_hi; b_a = n_a; b_b = n_b; }
    }
  }
  __syncthreads();
  f32x4* redc = (f32x4*)cb;
  f32x4 own[2];
#pragma unroll
  for (int g = 0; g < 4; g++) {
    f32x4 a = {T0[4 * g], T0[4 * g + 1], T0[4 * g + 2], T0[4 * g + 3]};
    f32x4 c = {T1[4 * g], T1[4 * g + 1], T1[4 * g + 2], T1[4 * g + 3]};
    if (g == w) { own[0] = a; own[1] = c; }
    else {
      if (g > w) redc[((w * 2 + 0) * 4 + g) * 64 + lane] = a;
      redc[((w * 2 + 1) * 4 + g) * 64 + lane] = c;
    }
  }
  __syncthreads();
#pragma unroll
  for (int T = 0; T < 2; T++) {
    f32x4 y = own[T];
#pragma unroll
    for (int ww = 0; ww < 4; ww++)
      if (ww != w && (T == 1 || ww < w)) y += redc[((ww * 2 + T) * 4 + w) * 64 + lane];
    const int l = lo0 + 1024 * T;
    const u32x2 ur = *((const u32x2*)(cb + CV_UOFF + 31 + (l >> 3)) + h);
    float uu[4];
    uu[0] = (float)__builtin_bit_cast(_Float16, (u16)(ur[0] & 0xffffu)) * (1.0f / 512.0f);
    uu[1] = (float)__builtin_bit_cast(_Float16, (u16)(ur[0] >> 16)) * (1.0f / 512.0f);
    uu[2] = (float)__builtin_bit_cast(_Float16, (u16)(ur[1] & 0xffffu)) * (1.0f / 512.0f);
    uu[3] = (float)__builtin_bit_cast(_Float16, (u16)(ur[1] >> 16)) * (1.0f / 512.0f);
    const u32x2 xo = T ? xo1 : xo0;
    float xx[4] = {lo16(xo[0]), hi16(xo[0]), lo16(xo[1]), hi16(xo[1])};
    float r[4];
#pragma unroll
    for (int e = 0; e < 4; e++) {
      float z = rbf(rbf(y[e] * kinv) + rbf(uu[e] * dsk));
      r[e] = xx[e] + z;
    }
    u32x2 o = {pk2(r[0], r[1]), pk2(r[2], r[3])};
    if (valid) *(u32x2*)(outT + (size_t)d * L_ + l) = o;
  }
}

template <int MODE>
__global__ __launch_bounds__(MODE == 0 ? 704 : 640) void k_gemm(const u16* __restrict__ XT, const u32x4* __restrict__ Wp,
                                                                const u16* __restrict__ resT, u16* __restrict__ out,
                                                                char* __restrict__ ws, f32x2* __restrict__ stout) {
  constexpr int NT = (MODE == 0) ? 11 : 10;
  constexpr int NTH = NT * 64;
  __shared__ __attribute__((aligned(16))) char smem[40960 + 32 * 164 * 4];
  const int t = threadIdx.x, lane = t & 63, w = __builtin_amdgcn_readfirstlane(t >> 6);
  const int b = blockIdx.x;
  const int cg = (b >> 3) & 3, rb = (b & 7) + 8 * (b >> 5);
  const int l0 = rb * 32;
  const int g = lane >> 4, li = lane & 15;
  const int ntile = cg * NT + w;
  u32x4 xv[4];
#pragma unroll
  for (int i = 0; i < 4; i++) {
    const int c = t + NTH * i;
    if (c < 2560) xv[i] = *(const u32x4*)(XT + (size_t)(c >> 2) * L_ + l0 + (c & 3) * 8);
  }
  const u32x4* wp0 = Wp + (size_t)ntile * 20 * 64 + lane;
  u32x4 wf[20];
#pragma unroll
  for (int p = 0; p < 3; p++) wf[p] = wp0[p * 64];
  __builtin_amdgcn_sched_barrier(0);
#pragma unroll
  for (int i = 0; i < 4; i++) {
    const int c = t + NTH * i;
    const int row = c >> 2;
    if (c < 2560) *(u32x4*)(smem + row * 64 + (((c & 3) ^ (2 * ((row >> 3) & 1))) * 16)) = xv[i];
  }
  __syncthreads();
  f32x4 acc[2] = {{0, 0, 0, 0}, {0, 0, 0, 0}};
  const int q4 = li >> 2, p4 = li & 3;
  const unsigned sbase = (unsigned)(size_t)smem;
  unsigned xa[2];
#pragma unroll
  for (int lt = 0; lt < 2; lt++)
    xa[lt] = sbase + (8 * g + q4) * 64 + (((2 * lt + (p4 >> 1)) ^ (2 * (g & 1))) * 16) + 8 * (p4 & 1);
  f32x2* stp = (f32x2*)(smem + 40960);
  if (MODE == 0) {
#pragma unroll
    for (int p = 3; p < 9; p++) wf[p] = wp0[p * 64];
    __builtin_amdgcn_sched_barrier(0);
    float ssum[2] = {0.f, 0.f}, ssq[2] = {0.f, 0.f};
    const bf16x2 ones = __builtin_bit_cast(bf16x2, 0x3F803F80u);
#pragma unroll
    for (int kk = 0; kk < 2; kk++) {
      const int ks = w + NT * kk;
      if (ks < 20) {
#pragma unroll
        for (int lt = 0; lt < 2; lt++) {
          const u32x4 xf = tr_frag(xa[lt] + ks * 2048, xa[lt] + ks * 2048 + 256);
#pragma unroll
          for (int e = 0; e < 4; e++) {
            const unsigned ux = xf[e];
            bf16x2 xx = __builtin_bit_cast(bf16x2, ux);
            ssum[lt] = __builtin_amdgcn_fdot2_f32_bf16(xx, ones, ssum[lt], false);
            ssq[lt] = __builtin_amdgcn_fdot2_f32_bf16(xx, xx, ssq[lt], false);
          }
        }
      }
    }
#pragma unroll
    for (int lt = 0; lt < 2; lt++) {
      ssum[lt] += __shfl_xor(ssum[lt], 16); ssum[lt] += __shfl_xor(ssum[lt], 32);
      ssq[lt] += __shfl_xor(ssq[lt], 16); ssq[lt] += __shfl_xor(ssq[lt], 32);
      if (g == 0) { f32x2 o = {ssum[lt], ssq[lt]}; stp[w * 32 + 16 * lt + li] = o; }
    }
  }
  __builtin_amdgcn_sched_barrier(0);
#pragma unroll
  for (int p = (MODE == 0) ? 9 : 3; p < 20; p++) wf[p] = wp0[p * 64];
  f32x4 c4;
  u32x2 xr[2];
  u16 xs[2][4];
  if (MODE == 0) c4 = *(const f32x4*)((const float*)(ws + OFF_CS) + ntile * 16 + 4 * g);
  if (MODE == 1) {
#pragma unroll
    for (int lt = 0; lt < 2; lt++) xr[lt] = *(const u32x2*)(resT + (size_t)(ntile * 16 + li) * L_ + l0 + 16 * lt + 4 * g);
  }
  if (MODE == 2) {
#pragma unroll
    for (int lt = 0; lt < 2; lt++)
#pragma unroll
      for (int e = 0; e < 4; e++) xs[lt][e] = resT[(size_t)(ntile * 16 + 4 * g + e) * L_ + l0 + 16 * lt + li];
  }
#pragma unroll
  for (int ks = 0; ks < 20; ks++) {
    u32x4 xf[2];
#pragma unroll
    for (int lt = 0; lt < 2; lt++) xf[lt] = tr_frag(xa[lt] + ks * 2048, xa[lt] + ks * 2048 + 256);
    const bf16x8 wv = __builtin_bit_cast(bf16x8, wf[ks]);
#pragma unroll
    for (int lt = 0; lt < 2; lt++) {
      if (MODE == 1) acc[lt] = __builtin_amdgcn_mfma_f32_16x16x32_bf16(__builtin_bit_cast(bf16x8, xf[lt]), wv, acc[lt], 0, 0, 0);
      else acc[lt] = __builtin_amdgcn_mfma_f32_16x16x32_bf16(wv, __builtin_bit_cast(bf16x8, xf[lt]), acc[lt], 0, 0, 0);
    }
  }
  if (MODE == 0) {
    __syncthreads();
    f32x2 svv[2];
#pragma unroll
    for (int lt = 0; lt < 2; lt++) {
      f32x2 a = {0.f, 0.f};
#pragma unroll
      for (int i = 0; i < 11; i++) a += stp[i * 32 + 16 * lt + li];
      float mean = a[0] * (1.0f / 640.0f);
      float var = fmaxf(a[1] * (1.0f / 640.0f) - mean * mean, 0.f);
      float sd = rbf(__builtin_amdgcn_sqrtf(rbf(rbf(var) + 1e-5f)));
      svv[lt] = (f32x2){rbf(mean), __builtin_amdgcn_rcpf(sd)};
    }
    u16* qo = (u16*)(ws + OFF_Q);
    char* kvimg = ws + OFF_KV;
    const int n0 = ntile * 16 + 4 * g;
#pragma unroll
    for (int lt = 0; lt < 2; lt++) {
      const f32x2 sv = svv[lt];
      const int l = l0 + 16 * lt + li;
      float v[4];
#pragma unroll
      for (int r = 0; r < 4; r++) v[r] = (acc[lt][r] - sv[0] * c4[r]) * sv[1];
      if (n0 < 640) {
        u32x2 o = {pk2(v[0] * QSCALE, v[1] * QSCALE), pk2(v[2] * QSCALE, v[3] * QSCALE)};
        *(u32x2*)(qo + (size_t)l * D_ + n0) = o;
      } else if (n0 < 660) {
        const int dh = n0 - 640;
        u32x2 o = {pk2(v[0], v[1]), pk2(v[2], v[3])};
        const int tile = l >> 5, ln = (l & 31) + 32 * ((dh & 15) >> 3);
        if (dh < 16) *(u32x2*)(kvimg + tile * KV_TB + ln * 16 + (dh & 7) * 2) = o;
        else *(u32x2*)(kvimg + tile * KV_TB + KV_K1 + (l & 31) * 8) = o;
      } else if (n0 < 680) {
        const int dv = n0 - 660;
        const int tile = l >> 5, kk = l & 15, step = (l & 31) >> 4;
        const int hh = (kk & 7) >> 2, jj = (kk >> 3) * 4 + (kk & 3);
#pragma unroll
        for (int r = 0; r < 4; r++)
          *(u16*)(kvimg + tile * KV_TB + KV_V0 + step * 672 + hh * 336 + (dv + r) * 16 + jj * 2) = f2bf(v[r]);
      }
    }
  } else if (MODE == 1) {
    float* stg = (float*)(smem + 40960);
    const int nl = w * 16 + li;
    const int n = cg * 160 + nl;
#pragma unroll
    for (int lt = 0; lt < 2; lt++) {
      const int ll = 16 * lt + 4 * g;
      float r[4];
      r[0] = rbf(lo16(xr[lt][0]) + rbf(acc[lt][0]));
      r[1] = rbf(hi16(xr[lt][0]) + rbf(acc[lt][1]));
      r[2] = rbf(lo16(xr[lt][1]) + rbf(acc[lt][2]));
      r[3] = rbf(hi16(xr[lt][1]) + rbf(acc[lt][3]));
      u32x2 o = {pk2(r[0], r[1]), pk2(r[2], r[3])};
      *(u32x2*)(out + (size_t)n * L_ + l0 + ll) = o;
#pragma unroll
      for (int e = 0; e < 4; e++) stg[(ll + e) * 164 + nl] = r[e];
    }
    __syncthreads();
    if (t < 256) {
      const int row = t >> 3, part = t & 7;
      float v[20];
      float s = 0.f;
#pragma unroll
      for (int i = 0; i < 5; i++) {
        f32x4 a = *(const f32x4*)(stg + row * 164 + part * 20 + 4 * i);
#pragma unroll
        for (int e = 0; e < 4; e++) { v[4 * i + e] = a[e]; s += a[e]; }
      }
      s += __shfl_xor(s, 1); s += __shfl_xor(s, 2); s += __shfl_xor(s, 4);
      const float mean = s * (1.0f / 160.0f);
      float m2 = 0.f;
#pragma unroll
      for (int i = 0; i < 20; i++) { float a = v[i] - mean; m2 += a * a; }
      m2 += __shfl_xor(m2, 1); m2 += __shfl_xor(m2, 2); m2 += __shfl_xor(m2, 4);
      if (part == 0) { f32x2 o = {mean, m2}; stout[cg * L_ + l0 + row] = o; }
    }
  } else {
    const int n0 = ntile * 16 + 4 * g;
#pragma unroll
    for (int lt = 0; lt < 2; lt++) {
      const int l = l0 + 16 * lt + li;
      float r[4];
#pragma unroll
      for (int e = 0; e < 4; e++) r[e] = bf2f(xs[lt][e]) + rbf(acc[lt][e]);
      u32x2 o = {pk2(r[0], r[1]), pk2(r[2], r[3])};
      *(u32x2*)(out + (size_t)l * D_ + n0) = o;
    }
  }
}

__device__ __forceinline__ void pack_p(const f32x16& s, bf16x8& p0, bf16x8& p1) {
  u32x4 a, c;
#pragma unroll
  for (int i = 0; i < 4; i++) {
    a[i] = pk2(__builtin_amdgcn_exp2f(s[2 * i]), __builtin_amdgcn_exp2f(s[2 * i + 1]));
    c[i] = pk2(__builtin_amdgcn_exp2f(s[8 + 2 * i]), __builtin_amdgcn_exp2f(s[9 + 2 * i]));
  }
  p0 = __builtin_bit_cast(bf16x8, a);
  p1 = __builtin_bit_cast(bf16x8, c);
}
__device__ __forceinline__ float max16(const f32x16& s) {
  float m = fmaxf(s[0], s[1]);
#pragma unroll
  for (int i = 2; i < 16; i++) m = fmaxf(m, s[i]);
  return m;
}

__device__ __forceinline__ u32x4 k1frag(u32x2 v) { return (u32x4){v[0], v[1], 0u, 0u}; }

template <int MODE>
__global__ __launch_bounds__(1024) void k_attn(const char* __restrict__ ws_q, const char* __restrict__ kv,
                                               u16* oT, const u32x4* __restrict__ Wp,
                                               const u16* __restrict__ resT, u16* __restrict__ out,
                                               f32x2* __restrict__ stout, unsigned* cnt) {
  __shared__ __attribute__((aligned(16))) char sm[62 * KV_TB];
  const int t = threadIdx.x, lane = t & 63, w = __builtin_amdgcn_readfirstlane(t >> 6);
  const int b = blockIdx.x;
  const int kh = w >> 3;
  const int pb = (b & 7) + 8 * (b >> 5), head = ((b >> 3) & 3) * 8 + (w & 7);
  const int ci = lane & 31, h = lane >> 5;
  const int l = pb * 32 + ci;
#pragma unroll
  for (int hh = 0; hh < 2; hh++)
    for (int i = w * 64; i < 8 * 164; i += 1024)
      if (i + lane < 8 * 164)
        __builtin_amdgcn_global_load_lds((const __attribute__((address_space(1))) void*)(kv + (size_t)hh * 32 * KV_TB + (i + lane) * 16),
                                         (LDS_AS void*)(sm + hh * 31 * KV_TB + (i + lane) * 16), 16, 0, 0);
  bf16x8 q0, q1;
  s16x4 q1s;
  {
    const char* qp = ws_q + (size_t)l * (D_ * 2) + head * 40;
    u32x2 a = *(const u32x2*)(qp + 16 * h), c = *(const u32x2*)(qp + 16 * h + 8);
    u32x2 e = *(const u32x2*)(qp + 32);
    u32x4 f0 = {a[0], a[1], c[0], c[1]};
    u32x4 f1 = {h ? 0u : e[0], h ? 0u : e[1], 0u, 0u};
    q0 = __builtin_bit_cast(bf16x8, f0);
    q1 = __builtin_bit_cast(bf16x8, f1);
    u32x2 f1s = {f1[0], f1[1]};
    q1s = __builtin_bit_cast(s16x4, f1s);
  }
  __syncthreads();
#define DMA_B(j)                                                                                                      \
  {                                                                                                                   \
    const int hh_ = (j) & 1, i_ = 8 * 164 + w * 64 + ((j) >> 1) * 1024;                                               \
    if (i_ + lane < 31 * 164)                                                                                         \
      __builtin_amdgcn_global_load_lds((const __attribute__((address_space(1))) void*)(kv + (size_t)hh_ * 32 * KV_TB + (i_ + lane) * 16), \
                                       (LDS_AS void*)(sm + hh_ * 31 * KV_TB + (i_ + lane) * 16), 16, 0, 0);          \
  }
  DMA_B(0) DMA_B(1)
  const int ok0 = lane * 16, ok1 = KV_K1 + ci * 8, ov0 = KV_V0 + h * 336 + min(ci, 20) * 16, ov1 = ov0 + 672;
  f32x16 negm;
  {
    f32x16 z = {0};
    f32x16 s = __builtin_amdgcn_mfma_f32_32x32x16_bf16(*(const bf16x8*)(sm + ok0), q0, z, 0, 0, 0);
    s = __builtin_amdgcn_mfma_f32_32x32x16_bf16(__builtin_bit_cast(bf16x8, k1frag(*(const u32x2*)(sm + ok1))), q1, s, 0, 0, 0);
    float m = max16(s);
    m = fmaxf(m, __shfl_xor(m, 32));
#pragma unroll
    for (int i = 0; i < 16; i++) negm[i] = -m;
  }
  f32x16 o = {0};
#define SB() __builtin_amdgcn_sched_barrier(0)
  const char* base = sm + kh * 31 * KV_TB;
  const char* gl = kv + (size_t)(kh * 32 + 31) * KV_TB;
  u32x4 k0n = *(const u32x4*)(base + ok0);
  u32x2 k1n = *(const u32x2*)(base + ok1);
  u32x4 v0 = *(const u32x4*)(base + ov0), v1 = *(const u32x4*)(base + ov1);
  f32x16 s = __builtin_amdgcn_mfma_f32_32x32x16_bf16(__builtin_bit_cast(bf16x8, k0n), q0, negm, 0, 0, 0);
  s = __builtin_amdgcn_mfma_f32_32x32x16_bf16(__builtin_bit_cast(bf16x8, k1frag(k1n)), q1, s, 0, 0, 0);
  k0n = *(const u32x4*)(base + KV_TB + ok0);
  k1n = *(const u32x2*)(base + KV_TB + ok1);
#define TILE(KP, VP, NEXT)                                                                                            \
  {                                                                                                                   \
    u32x4 pa, pb;                                                                                                     \
    f32x16 sn;                                                                                                        \
    if (NEXT) sn = __builtin_amdgcn_mfma_f32_32x32x16_bf16(__builtin_bit_cast(bf16x8, k0n), q0, negm, 0, 0, 0);       \
    SB();                                                                                                             \
    pa[0] = pk2(__builtin_amdgcn_exp2f(s[0]), __builtin_amdgcn_exp2f(s[1]));                                          \
    pa[1] = pk2(__builtin_amdgcn_exp2f(s[2]), __builtin_amdgcn_exp2f(s[3]));                                          \
    SB();                                                                                                             \
    if (NEXT) sn = __builtin_amdgcn_mfma_f32_32x32x8bf16_1k(__builtin_bit_cast(s16x4, k1n), q1s, sn, 0, 0, 0);       \
    u32x4 v0n = v0, v1n = v1;                                                                                         \
    if (NEXT) { k0n = *(const u32x4*)((KP) + ok0); k1n = *(const u32x2*)((KP) + ok1); }                               \
    if (NEXT) { v0n = *(const u32x4*)((VP) + ov0); v1n = *(const u32x4*)((VP) + ov1); }                               \
    SB();                                                                                                             \
    pa[2] = pk2(__builtin_amdgcn_exp2f(s[4]), __builtin_amdgcn_exp2f(s[5]));                                          \
    pa[3] = pk2(__builtin_amdgcn_exp2f(s[6]), __builtin_amdgcn_exp2f(s[7]));                                          \
    pb[0] = pk2(__builtin_amdgcn_exp2f(s[8]), __builtin_amdgcn_exp2f(s[9]));                                          \
    SB();                                                                                                             \
    o = __builtin_amdgcn_mfma_f32_32x32x16_bf16(__builtin_bit_cast(bf16x8, v0), __builtin_bit_cast(bf16x8, pa), o, 0, 0, 0); \
    SB();                                                                                                             \
    pb[1] = pk2(__builtin_amdgcn_exp2f(s[10]), __builtin_amdgcn_exp2f(s[11]));                                        \
    pb[2] = pk2(__builtin_amdgcn_exp2f(s[12]), __builtin_amdgcn_exp2f(s[13]));                                        \
    pb[3] = pk2(__builtin_amdgcn_exp2f(s[14]), __builtin_amdgcn_exp2f(s[15]));                                        \
    SB();                                                                                                             \
    o = __builtin_amdgcn_mfma_f32_32x32x16_bf16(__builtin_bit_cast(bf16x8, v1), __builtin_bit_cast(bf16x8, pb), o, 0, 0, 0); \
    v0 = v0n; v1 = v1n;                                                                                               \
    if (NEXT) s = sn;                                                                                                 \
    SB();                                                                                                             \
  }
  __builtin_amdgcn_s_setprio(3);
#pragma unroll
  for (int tl = 0; tl < 6; tl++) {
    if (tl < 3) { DMA_B(2 * tl + 2) DMA_B(2 * tl + 3) }
    TILE(base + (tl + 2) * KV_TB, base + (tl + 1) * KV_TB, true)
  }
  __syncthreads();
#pragma unroll 2
  for (int tl = 6; tl < 20; tl++) TILE(base + (tl + 2) * KV_TB, base + (tl + 1) * KV_TB, true)
  __builtin_amdgcn_s_setprio(2);
#pragma unroll 2
  for (int tl = 20; tl < 28; tl++) TILE(base + (tl + 2) * KV_TB, base + (tl + 1) * KV_TB, true)
  __builtin_amdgcn_s_setprio(1);
  TILE(base + 30 * KV_TB, base + 29 * KV_TB, true)
  TILE(gl, base + 30 * KV_TB, true)
  TILE(gl, gl, true)
  __builtin_amdgcn_s_setprio(0);
  TILE(gl, gl, false)
  const int cg = (b >> 3) & 3, l0 = pb * 32;
  const int g = lane >> 4, li = lane & 15;
  const bool act = w < 10;
  const int ntile = cg * 10 + (act ? w : 0);
  u32x4 wf[20];
  u32x2 xr[2];
  u16 xs[2][4];
  const u32x4* wp0 = Wp + (size_t)ntile * 20 * 64 + lane;
  if (act) {
#pragma unroll
    for (int p = 0; p < 6; p++) wf[p] = wp0[p * 64];
    if (MODE == 1) {
#pragma unroll
      for (int lt = 0; lt < 2; lt++) xr[lt] = *(const u32x2*)(resT + (size_t)(ntile * 16 + li) * L_ + l0 + 16 * lt + 4 * g);
    } else {
#pragma unroll
      for (int lt = 0; lt < 2; lt++)
#pragma unroll
        for (int e = 0; e < 4; e++) xs[lt][e] = resT[(size_t)(ntile * 16 + 4 * g + e) * L_ + l0 + 16 * lt + li];
    }
  }
  __syncthreads();
  f32x4* mg = (f32x4*)(sm + 65536);
  if (kh == 1) {
#pragma unroll
    for (int i = 0; i < 3; i++) {
      f32x4 a = {o[4 * i], o[4 * i + 1], o[4 * i + 2], o[4 * i + 3]};
      mg[((w & 7) * 3 + i) * 64 + lane] = a;
    }
  }
  __syncthreads();
  if (kh == 0) {
#pragma unroll
    for (int i = 0; i < 3; i++) {
      f32x4 a = mg[(w * 3 + i) * 64 + lane];
#pragma unroll
      for (int e = 0; e < 4; e++) o[4 * i + e] += a[e];
    }
    float lsum = __shfl(o[8], ci + 32);
    bool bad = !(lsum <= 1.8446744e19f);
#pragma unroll
    for (int i = 0; i < 12; i++) bad = bad || !(fabsf(o[i]) <= 3.0e38f);
    if (__any(bad)) {
      float m = -INFINITY;
#pragma unroll
      for (int i = 0; i < 16; i++) o[i] = 0.f;
      for (int tl = 0; tl < 64; tl++) {
        const char* tp = kv + (size_t)tl * KV_TB;
        f32x16 z = {0};
        f32x16 s2 = __builtin_amdgcn_mfma_f32_32x32x16_bf16(*(const bf16x8*)(tp + ok0), q0, z, 0, 0, 0);
        s2 = __builtin_amdgcn_mfma_f32_32x32x16_bf16(__builtin_bit_cast(bf16x8, k1frag(*(const u32x2*)(tp + ok1))), q1, s2, 0, 0, 0);
        float tm = max16(s2);
        tm = fmaxf(tm, __shfl_xor(tm, 32));
        const float mn = fmaxf(m, tm);
        const float al = __builtin_amdgcn_exp2f(m - mn);
        m = mn;
#pragma unroll
        for (int i = 0; i < 16; i++) { o[i] *= al; s2[i] -= mn; }
        bf16x8 p0, p1;
        pack_p(s2, p0, p1);
        o = __builtin_amdgcn_mfma_f32_32x32x16_bf16(*(const bf16x8*)(tp + ov0), p0, o, 0, 0, 0);
        o = __builtin_amdgcn_mfma_f32_32x32x16_bf16(*(const bf16x8*)(tp + ov1), p1, o, 0, 0, 0);
      }
      lsum = __shfl(o[8], ci + 32);
    }
    const float inv = 1.0f / lsum;
#pragma unroll
    for (int i = 0; i < 12; i++) {
      const int row = head * 20 + 4 * h + (i & 3) + 8 * (i >> 2);
      if (i < 8 || h == 0)
        *(u16*)(sm + row * 64 + (((ci >> 3) ^ (2 * ((row >> 3) & 1))) * 16) + (ci & 7) * 2) = f2bf(o[i] * inv);
    }
  }
  __syncthreads();
  const __amdgpu_buffer_rsrc_t orsrc = __builtin_amdgcn_make_buffer_rsrc((void*)oT, 0, (int)SZ_ACT, 0x00020000);
  if (t < 640) {
    const int row = cg * 160 + (t >> 2), c = t & 3;
    const u32x4 v = *(const u32x4*)(sm + row * 64 + ((c ^ (2 * ((row >> 3) & 1))) * 16));
    __builtin_amdgcn_raw_buffer_store_b128(v, orsrc, (unsigned)((row * L_ + l0 + c * 8) * 2), 0, 16);
  }
  asm volatile("s_waitcnt vmcnt(0)" ::: "memory");
  __syncthreads();
  if (act && w > 0) {
#pragma unroll
    for (int p = 6; p < 20; p++) wf[p] = wp0[p * 64];
  }
  if (t == 0) {
    __hip_atomic_fetch_add(cnt + pb * 64, 1u, __ATOMIC_RELAXED, __HIP_MEMORY_SCOPE_AGENT);
    for (unsigned spins = 0; spins < (1u << 24); spins++) {
      if (__hip_atomic_load(cnt + pb * 64, __ATOMIC_RELAXED, __HIP_MEMORY_SCOPE_AGENT) >= 4u) break;
      __builtin_amdgcn_s_sleep(1);
    }
  }
  __builtin_amdgcn_fence(__ATOMIC_ACQUIRE, "wavefront");
  if (w == 0) {
#pragma unroll
    for (int p = 6; p < 20; p++) wf[p] = wp0[p * 64];
  }
  __syncthreads();
  {
    u32x4 v[2];
#pragma unroll
    for (int i = 0; i < 2; i++) {
      const int c = t + 1024 * i;
      int row = c >> 2;
      row += (row >= cg * 160) ? 160 : 0;
      if (c < 1920) v[i] = __builtin_amdgcn_raw_buffer_load_b128(orsrc, (unsigned)((row * L_ + l0 + (c & 3) * 8) * 2), 0, 16);
    }
#pragma unroll
    for (int i = 0; i < 2; i++) {
      const int c = t + 1024 * i;
      int row = c >> 2;
      row += (row >= cg * 160) ? 160 : 0;
      if (c < 1920) *(u32x4*)(sm + row * 64 + (((c & 3) ^ (2 * ((row >> 3) & 1))) * 16)) = v[i];
    }
  }
  __syncthreads();
  f32x4 acc[2] = {{0, 0, 0, 0}, {0, 0, 0, 0}};
  if (act) {
    const int q4 = li >> 2, p4 = li & 3;
    const unsigned sbase = (unsigned)(size_t)sm;
    unsigned xa[2];
#pragma unroll
    for (int lt = 0; lt < 2; lt++)
      xa[lt] = sbase + (8 * g + q4) * 64 + (((2 * lt + (p4 >> 1)) ^ (2 * (g & 1))) * 16) + 8 * (p4 & 1);
#pragma unroll
    for (int ks = 0; ks < 20; ks++) {
      const bf16x8 wv = __builtin_bit_cast(bf16x8, wf[ks]);
#pragma unroll
      for (int lt = 0; lt < 2; lt++) {
        const u32x4 xf = tr_frag(xa[lt] + ks * 2048, xa[lt] + ks * 2048 + 256);
        if (MODE == 1) acc[lt] = __builtin_amdgcn_mfma_f32_16x16x32_bf16(__builtin_bit_cast(bf16x8, xf), wv, acc[lt], 0, 0, 0);
        else acc[lt] = __builtin_amdgcn_mfma_f32_16x16x32_bf16(wv, __builtin_bit_cast(bf16x8, xf), acc[lt], 0, 0, 0);
      }
    }
  }
  if (MODE == 1) {
    float* stg = (float*)(sm + 40960);
    if (act) {
      const int nl = w * 16 + li;
      const int n = cg * 160 + nl;
#pragma unroll
      for (int lt = 0; lt < 2; lt++) {
        const int ll = 16 * lt + 4 * g;
        float r[4];
        r[0] = rbf(lo16(xr[lt][0]) + rbf(acc[lt][0]));
        r[1] = rbf(hi16(xr[lt][0]) + rbf(acc[lt][1]));
        r[2] = rbf(lo16(xr[lt][1]) + rbf(acc[lt][2]));
        r[3] = rbf(hi16(xr[lt][1]) + rbf(acc[lt][3]));
        u32x2 ov = {pk2(r[0], r[1]), pk2(r[2], r[3])};
        *(u32x2*)(out + (size_t)n * L_ + l0 + ll) = ov;
#pragma unroll
        for (int e = 0; e < 4; e++) stg[(ll + e) * 164 + nl] = r[e];
      }
    }
    __syncthreads();
    if (t < 256) {
      const int row = t >> 3, part = t & 7;
      float v[20];
      float sum = 0.f;
#pragma unroll
      for (int i = 0; i < 5; i++) {
        f32x4 a = *(const f32x4*)(stg + row * 164 + part * 20 + 4 * i);
#pragma unroll
        for (int e = 0; e < 4; e++) { v[4 * i + e] = a[e]; sum += a[e]; }
      }
      sum += __shfl_xor(sum, 1); sum += __shfl_xor(sum, 2); sum += __shfl_xor(sum, 4);
      const float mean = sum * (1.0f / 160.0f);
      float m2 = 0.f;
#pragma unroll
      for (int i = 0; i < 20; i++) { float a = v[i] - mean; m2 += a * a; }
      m2 += __shfl_xor(m2, 1); m2 += __shfl_xor(m2, 2); m2 += __shfl_xor(m2, 4);
      if (part == 0) { f32x2 ov = {mean, m2}; stout[cg * L_ + l0 + row] = ov; }
    }
  } else if (act) {
    const int n0 = ntile * 16 + 4 * g;
#pragma unroll
    for (int lt = 0; lt < 2; lt++) {
      const int lq = l0 + 16 * lt + li;
      float r[4];
#pragma unroll
      for (int e = 0; e < 4; e++) r[e] = bf2f(xs[lt][e]) + rbf(acc[lt][e]);
      u32x2 ov = {pk2(r[0], r[1]), pk2(r[2], r[3])};
      *(u32x2*)(out + (size_t)lq * D_ + n0) = ov;
    }
  }
}

extern "C" void kernel_launch(void* const* d_in, const int* in_sizes, int n_in, void* d_out, int out_size,
                              void* d_ws, size_t ws_size, hipStream_t stream) {
  const u16* x = (const u16*)d_in[0];
  const u16* logA = (const u16*)d_in[1];
  const u16* Aim = (const u16*)d_in[2];
  const u16* Cre = (const u16*)d_in[3];
  const u16* Cim = (const u16*)d_in[4];
  const u16* logdt = (const u16*)d_in[5];
  const u16* dskip = (const u16*)d_in[6];
  const u16* wq = (const u16*)d_in[8];
  const u16* wk = (const u16*)d_in[9];
  const u16* wv = (const u16*)d_in[10];
  const u16* wo = (const u16*)d_in[11];
  char* ws = (char*)d_ws;
  u16* xt0 = (u16*)(ws + OFF_XT0);
  u16* x1t = (u16*)(ws + OFF_X1T);
  u16* x2t = (u16*)(ws + OFF_X2T);
  u16* x3t = (u16*)(ws + OFF_X3T);
  u16* ot = (u16*)(ws + OFF_OT);
  const u32x4* wqkv = (const u32x4*)(ws + OFF_WQKV);
  const u32x4* wop = (const u32x4*)(ws + OFF_WO);
  const char* kv = ws + OFF_KV;
  f32x2* st0 = (f32x2*)(ws + OFF_ST0);
  f32x2* st1 = (f32x2*)(ws + OFF_ST1);

  k_prep<<<PREP_WGS, 1024, 0, stream>>>(x, logA, Aim, Cre, Cim, logdt, wq, wk, wv, wo, ws);
  k_conv<true><<<214, 768, 0, stream>>>(xt0, st0, ws, dskip, x1t);
  k_gemm<0><<<256, 704, 0, stream>>>(x1t, wqkv, nullptr, nullptr, ws, nullptr);
  k_attn<1><<<256, 1024, 0, stream>>>(ws + OFF_Q, kv, ot, wop, x1t, x2t, st1, (unsigned*)(ws + OFF_CNT));
  k_conv<false><<<214, 768, 0, stream>>>(x2t, st1, ws, dskip, x3t);
  k_gemm<0><<<256, 704, 0, stream>>>(x3t, wqkv, nullptr, nullptr, ws, nullptr);
  k_attn<2><<<256, 1024, 0, stream>>>(ws + OFF_Q, kv, ot, wop, x3t, (u16*)d_out, nullptr, (unsigned*)(ws + OFF_CNT + 64 * 256));
}
```

```cpp
#include <hip/hip_runtime.h>
#include <hip/hip_bf16.h>
#include <stdint.h>

typedef unsigned short u16;
typedef __attribute__((ext_vector_type(8))) __bf16 bf16x8;
typedef __attribute__((ext_vector_type(8))) _Float16 f16x8;
typedef __attribute__((ext_vector_type(16))) float f32x16;
typedef __attribute__((ext_vector_type(4))) float f32x4;
typedef __attribute__((ext_vector_type(4))) unsigned u32x4;
typedef __attribute__((ext_vector_type(2))) unsigned u32x2;
typedef __attribute__((ext_vector_type(4))) short s16x4;
typedef __attribute__((ext_vector_type(2))) float f32x2;

#define LDS_AS __attribute__((address_space(3)))

constexpr int L_ = 2048, D_ = 640, NS_ = 128;
constexpr int KFROW = 2304;
constexpr int KV_TB = 2624, KV_K1 = 1024, KV_V0 = 1280, KV_V1 = 1952;
constexpr float LOG2E = 1.4426950408889634f;
constexpr float INV2PI = 0.15915494309189535f;
constexpr float QSCALE = 0.22360679774997896f * 1.4426950408889634f;

constexpr size_t SZ_ACT = (size_t)D_ * L_ * 2;
constexpr size_t OFF_XT0 = 0;
constexpr size_t OFF_X1T = OFF_XT0 + SZ_ACT;
constexpr size_t OFF_X2T = OFF_X1T + SZ_ACT;
constexpr size_t OFF_X3T = OFF_X2T + SZ_ACT;
constexpr size_t OFF_Q = OFF_X3T + SZ_ACT;
constexpr size_t OFF_OT = OFF_Q + SZ_ACT;
constexpr size_t OFF_KF = OFF_OT + SZ_ACT;
constexpr size_t OFF_KINV = OFF_KF + (size_t)D_ * KFROW * 2;
constexpr size_t OFF_WQKV = OFF_KINV + 4096;
constexpr size_t OFF_WO = OFF_WQKV + (size_t)44 * 20 * 1024;
constexpr size_t OFF_CS = OFF_WO + (size_t)40 * 20 * 1024;
constexpr size_t OFF_KV = OFF_CS + 4096;
constexpr size_t OFF_ST0 = OFF_KV + 262144;
constexpr size_t OFF_ST1 = OFF_ST0 + 65536;

__device__ __forceinline__ float bf2f(u16 v) { return __uint_as_float(((unsigned)v) << 16); }
__device__ __forceinline__ u16 f2bf(float f) { __bf16 b = (__bf16)f; return __builtin_bit_cast(u16, b); }
__device__ __forceinline__ float rbf(float f) { return bf2f(f2bf(f)); }
typedef __attribute__((ext_vector_type(2))) __bf16 bf16x2;
__device__ __forceinline__ unsigned pk2(float a, float b) {
  f32x2 v = {a, b};
  return __builtin_bit_cast(unsigned, __builtin_convertvector(v, bf16x2));
}
__device__ __forceinline__ float lo16(unsigned v) { return __uint_as_float(v << 16); }
__device__ __forceinline__ float hi16(unsigned v) { return __uint_as_float(v & 0xffff0000u); }

constexpr int PREP_WGS = 256;
constexpr int PREP_UNIT_LDS = 23808;

__device__ __forceinline__ int hi_value(int idx) {
  int v;
  if (idx < 16) v = 128 * (idx >> 3) + (idx & 7);
  else if (idx < 24) v = 256 + 128 * ((idx - 16) >> 2) + 2 * ((idx - 16) & 3);
  else if (idx < 32) v = 512 + 128 * ((idx - 24) >> 1) + 4 * ((idx - 24) & 1);
  else if (idx <= 40) v = 1024 + 128 * (idx - 32);
  else v = 0;
  return v;
}

__device__ __forceinline__ u32x4 tr_frag(unsigned addr0, unsigned addr1) {
  s16x4 a = __builtin_amdgcn_ds_read_tr16_b64_v4i16((LDS_AS s16x4*)(size_t)addr0);
  s16x4 c = __builtin_amdgcn_ds_read_tr16_b64_v4i16((LDS_AS s16x4*)(size_t)addr1);
  u32x2 a2 = __builtin_bit_cast(u32x2, a), c2 = __builtin_bit_cast(u32x2, c);
  return (u32x4){a2[0], a2[1], c2[0], c2[1]};
}
__device__ __forceinline__ void split2(float a, float b, unsigned& hi, unsigned& lo) {
  hi = __builtin_bit_cast(unsigned, __builtin_amdgcn_cvt_pkrtz(a, b));
  float la, lb;
  asm("v_fma_mix_f32 %0, %1, -1.0, %2 op_sel_hi:[1,0,0]" : "=v"(la) : "v"(hi), "v"(a));
  asm("v_fma_mix_f32 %0, %1, -1.0, %2 op_sel:[1,0,0] op_sel_hi:[1,0,0]" : "=v"(lb) : "v"(hi), "v"(b));
  lo = __builtin_bit_cast(unsigned, __builtin_amdgcn_cvt_pkrtz(la, lb));
}

__global__ __launch_bounds__(1024) void k_prep(const u16* __restrict__ x, const u16* __restrict__ logA,
                                               const u16* __restrict__ Aim, const u16* __restrict__ Cre,
                                               const u16* __restrict__ Cim, const u16* __restrict__ logdt,
                                               const u16* __restrict__ wq, const u16* __restrict__ wk,
                                               const u16* __restrict__ wv, const u16* __restrict__ wo,
                                               char* __restrict__ ws) {
  __shared__ __attribute__((aligned(16))) char smem_all[4 * PREP_UNIT_LDS];
  const int ug = __builtin_amdgcn_readfirstlane(threadIdx.x >> 8);
  char* smem = smem_all + ug * PREP_UNIT_LDS;
  const int t = threadIdx.x & 255, lane = t & 63, w = __builtin_amdgcn_readfirstlane(t >> 6);
  const unsigned sbase = (unsigned)(size_t)smem;
  const int g = lane >> 4, li = lane & 15, q4 = li >> 2, p4 = li & 3;
  const int b = blockIdx.x;
  if (ug < 3 && b >= 214) {
    __syncthreads(); __syncthreads(); __syncthreads();
  } else if (ug < 3) {
    const int d = min(b * 3 + ug, D_ - 1);
    float* prm = (float*)smem;
    float* part = prm + 512;
    float* tab = part + 4 * 768;
    float* red = tab + 2312;
    if (t < 128) {
      const float dt = rbf(expf(bf2f(logdt[d])));
      float ar = -rbf(expf(bf2f(logA[d * NS_ + t])));
      float alpha = ar * dt, beta = bf2f(Aim[d * NS_ + t]) * dt;
      const float cr = 2.0f * (bf2f(Cre[d * NS_ + t]) * dt), ci = 2.0f * (bf2f(Cim[d * NS_ + t]) * dt);
      float bnd = hypotf(cr, ci);
      f32x4 pv = {alpha * LOG2E, beta * INV2PI, log2f(bnd), atan2f(ci, cr) * INV2PI};
      *(f32x4*)(prm + t * 4) = pv;
#pragma unroll
      for (int o = 32; o > 0; o >>= 1) bnd += __shfl_xor(bnd, o);
      if (lane == 0) red[w] = bnd;
    }
    __syncthreads();
    const float bnd = red[0] + red[1];
    int ex = (int)((__float_as_uint(bnd) >> 23) & 0xff) - 126;
    if (!(bnd > 1e-30f && bnd < 1e30f)) ex = 15;
    const float sA = (float)(14 - ex);
    const float lo = 8.0f * li;
    float hv[3] = {(float)hi_value(li), (float)hi_value(li + 16), (float)hi_value(li + 32)};
    f32x4 acc[3] = {{0, 0, 0, 0}, {0, 0, 0, 0}, {0, 0, 0, 0}};
#pragma unroll 1
    for (int ks = 0; ks < 2; ks++) {
      u32x4 bh, bl, ah[3], al[3];
#pragma unroll
      for (int m = 0; m < 4; m++) {
        const int n = 32 * w + 16 * ks + 4 * g + m;
        f32x4 p = *(const f32x4*)(prm + n * 4);
        p[2] += sA;
        {
          float e = __builtin_amdgcn_exp2f(fmaf(p[0], lo, 14.0f));
          float ph = __builtin_amdgcn_fractf(p[1] * lo);
          unsigned h_, l_;
          split2(e * __builtin_amdgcn_cosf(ph), e * __builtin_amdgcn_sinf(ph), h_, l_);
          bh[m] = h_; bl[m] = l_;
        }
#pragma unroll
        for (int tt = 0; tt < 3; tt++) {
          float e2 = __builtin_amdgcn_exp2f(fmaf(p[0], hv[tt], p[2]));
          float ph2 = __builtin_amdgcn_fractf(fmaf(p[1], hv[tt], p[3]));
          unsigned h_, l_;
          split2(e2 * __builtin_amdgcn_cosf(ph2), -e2 * __builtin_amdgcn_sinf(ph2), h_, l_);
          ah[tt][m] = h_; al[tt][m] = l_;
        }
      }
#pragma unroll
      for (int tt = 0; tt < 3; tt++) {
        acc[tt] = __builtin_amdgcn_mfma_f32_16x16x32_f16(__builtin_bit_cast(f16x8, al[tt]), __builtin_bit_cast(f16x8, bh), acc[tt], 0, 0, 0);
        acc[tt] = __builtin_amdgcn_mfma_f32_16x16x32_f16(__builtin_bit_cast(f16x8, ah[tt]), __builtin_bit_cast(f16x8, bl), acc[tt], 0, 0, 0);
        acc[tt] = __builtin_amdgcn_mfma_f32_16x16x32_f16(__builtin_bit_cast(f16x8, ah[tt]), __builtin_bit_cast(f16x8, bh), acc[tt], 0, 0, 0);
      }
    }
#pragma unroll
    for (int tt = 0; tt < 3; tt++)
#pragma unroll
      for (int r = 0; r < 4; r++) part[w * 768 + (16 * tt + g * 4 + r) * 16 + li] = acc[tt][r];
    __syncthreads();
#pragma unroll
    for (int k = 0; k < 3; k++) {
      const int i = t + 256 * k;
      const int hi = i >> 4, lo_i = i & 15;
      if (hi < 40 || (hi == 40 && lo_i == 0))
      {
        const int pp = hi_value(hi) + 8 * lo_i;
        tab[pp + (pp >> 3)] = (part[i] + part[768 + i]) + (part[1536 + i] + part[2304 + i]);
      }
    }
    __syncthreads();
    const float sc = 1.0f / 8192.0f;
    f16x8 o;
#pragma unroll
    for (int e = 0; e < 8; e++) {
      const int pp = (int)rbf((float)(8 * t + e));
      o[e] = (_Float16)(tab[pp + (pp >> 3)] * sc);
    }
    f16x8* dst = (f16x8*)(ws + OFF_KF + (size_t)d * KFROW * 2);
    dst[31 + t] = o;
    if (t < 32) {
      f16x8 z = {0, 0, 0, 0, 0, 0, 0, 0};
      dst[t < 31 ? t : 287] = z;
    }
    if (t == 0) ((float*)(ws + OFF_KINV))[d] = __uint_as_float((unsigned)(127 + ex - 15 - 9) << 23);
  } else if (b < 128) {
    __syncthreads();
    const int l0 = b * 16, row = t >> 4, part = t & 15;
    const u32x4* src = (const u32x4*)(x + (size_t)(l0 + row) * D_ + part * 40);
    u32x4 v[5];
#pragma unroll
    for (int i = 0; i < 5; i++) v[i] = src[i];
    if (t < 2) {
      const int c = b * 2 + t;
      const int tile = c >> 2, step = (c >> 1) & 1, hh = c & 1;
      u32x4 o = {0x3F803F80u, 0x3F803F80u, 0x3F803F80u, 0x3F803F80u};
      *(u32x4*)(ws + OFF_KV + tile * KV_TB + KV_V0 + step * 672 + hh * 336 + 320) = o;
    }
    float s = 0.f;
#pragma unroll
    for (int i = 0; i < 5; i++)
#pragma unroll
      for (int j = 0; j < 4; j++) s += lo16(v[i][j]) + hi16(v[i][j]);
#pragma unroll
    for (int o = 1; o < 16; o <<= 1) s += __shfl_xor(s, o);
    const float mean = s * (1.0f / 640.0f);
    float m2 = 0.f;
#pragma unroll
    for (int i = 0; i < 5; i++)
#pragma unroll
      for (int j = 0; j < 4; j++) {
        float a = lo16(v[i][j]) - mean, c = hi16(v[i][j]) - mean;
        m2 += a * a + c * c;
      }
#pragma unroll
    for (int o = 1; o < 16; o <<= 1) m2 += __shfl_xor(m2, o);
    if (part == 0) {
      const float vb = rbf(m2 * (1.0f / 640.0f));
      ((unsigned*)(ws + OFF_ST0))[l0 + row] = pk2(mean, __builtin_amdgcn_sqrtf(rbf(vb + 1e-5f)));
    }
#pragma unroll
    for (int i = 0; i < 5; i++) *(u32x4*)(smem + row * 1296 + part * 80 + i * 16) = v[i];
    __syncthreads();
    u16* xt = (u16*)(ws + OFF_XT0);
#pragma unroll
    for (int i = 0; i < 5; i++) {
      const int blk = (w * 5 + i) * 4 + g;
      const int ct = blk >> 1, rh = blk & 1;
      const unsigned ad = sbase + (rh * 8 + q4) * 1296 + ct * 32 + p4 * 8;
      const u32x4 f = tr_frag(ad, ad + 4 * 1296);
      *(u32x4*)(xt + (size_t)(ct * 16 + li) * L_ + l0 + rh * 8) = f;
    }
    __syncthreads();
  } else if (b < 208) {
    __syncthreads();
    const int u = b - 128;
    const int hf = u & 1, uu = u >> 1;
    const bool isq = uu < 20;
    const int ks = isq ? uu : uu - 20;
    const u16* W = isq ? wq : wo;
    u32x4* dst = (u32x4*)(ws + (isq ? OFF_WQKV : OFF_WO));
    u32x4 v[5];
#pragma unroll
    for (int i = 0; i < 5; i++) {
      const int c = t + 256 * i;
      const int r = c / 40, cc = c - r * 40;
      v[i] = *(const u32x4*)(W + (size_t)(ks * 32 + r) * 640 + hf * 320 + cc * 8);
    }
    const bool dokv = isq && hf == 0;
    u16 e[8];
    const int nkv = (40 + w) * 16 + li;
    if (dokv) {
#pragma unroll
      for (int j = 0; j < 8; j++) {
        const int k = ks * 32 + 8 * g + j;
        e[j] = (nkv < 660) ? wk[k * 20 + (nkv - 640)] : (nkv < 680) ? wv[k * 20 + (nkv - 660)] : (u16)0;
      }
    }
#pragma unroll
    for (int i = 0; i < 5; i++) {
      const int c = t + 256 * i;
      const int r = c / 40, cc = c - r * 40;
      *(u32x4*)(smem + r * 656 + cc * 16) = v[i];
    }
    __syncthreads();
#pragma unroll
    for (int i = 0; i < 5; i++) {
      const int nt = w * 5 + i;
      const unsigned ad = sbase + (8 * g + q4) * 656 + nt * 32 + p4 * 8;
      const u32x4 f = tr_frag(ad, ad + 4 * 656);
      dst[((size_t)(hf * 20 + nt) * 20 + ks) * 64 + lane] = f;
    }
    if (dokv) {
      u32x4 o;
#pragma unroll
      for (int j = 0; j < 4; j++) o[j] = (unsigned)e[2 * j] | ((unsigned)e[2 * j + 1] << 16);
      dst[((size_t)(40 + w) * 20 + ks) * 64 + lane] = o;
    }
    __syncthreads();
  } else if (b < 214) {
    __syncthreads(); __syncthreads(); __syncthreads();
  } else {
    __syncthreads();
    const int u = min(b - 214, 40);
    float sacc[8] = {0, 0, 0, 0, 0, 0, 0, 0};
    float* red = (float*)smem;
    if (u < 40) {
      const int c2 = t & 1, rg = t >> 1;
      const u32x4* src = (const u32x4*)(wq + (size_t)(rg * 5) * 640 + u * 16 + c2 * 8);
      u32x4 v[5];
#pragma unroll
      for (int i = 0; i < 5; i++) v[i] = src[i * 80];
#pragma unroll
      for (int i = 0; i < 5; i++)
#pragma unroll
        for (int j = 0; j < 4; j++) { sacc[2 * j] += lo16(v[i][j]); sacc[2 * j + 1] += hi16(v[i][j]); }
#pragma unroll
      for (int j = 0; j < 8; j++)
#pragma unroll
        for (int o = 2; o < 64; o <<= 1) sacc[j] += __shfl_xor(sacc[j], o);
      if (lane < 2) {
#pragma unroll
        for (int j = 0; j < 8; j++) red[w * 16 + lane * 8 + j] = sacc[j];
      }
    } else {
      const int c10 = t % 10, rg = t / 10;
      if (rg < 25) {
        const u16* src = ((c10 < 5) ? wk + c10 * 4 : wv + (c10 - 5) * 4);
#pragma unroll
        for (int i = 0; i < 26; i++) {
          const int row = rg + 25 * i;
          if (row < 640) {
            const u32x2 v = *(const u32x2*)(src + (size_t)row * 20);
            sacc[0] += lo16(v[0]); sacc[1] += hi16(v[0]); sacc[2] += lo16(v[1]); sacc[3] += hi16(v[1]);
          }
        }
#pragma unroll
        for (int j = 0; j < 4; j++) red[rg * 40 + c10 * 4 + j] = sacc[j];
      }
    }
    __syncthreads();
    if (u < 40) {
      if (t < 16) ((float*)(ws + OFF_CS))[u * 16 + t] = (red[t] + red[16 + t]) + (red[32 + t] + red[48 + t]);
    } else if (t < 64) {
      float v = 0.f;
      if (t < 40) {
#pragma unroll
        for (int i = 0; i < 25; i++) v += red[i * 40 + t];
      }
      ((float*)(ws + OFF_CS))[640 + t] = v;
    }
    __syncthreads();
  }
}

constexpr int CV_COPY = 321;
constexpr int CV_UOFF = 2576;
constexpr int CV_CH = CV_UOFF + 288;
constexpr int CV_ZERO = 3 * CV_CH;
constexpr int CV_STAT = CV_ZERO + 48;
constexpr int CV_TOTAL = CV_STAT + 512;

template <bool FINAL>
__global__ __launch_bounds__(768) void k_conv(const u16* __restrict__ xT, const f32x2* __restrict__ st,
                                              const char* __restrict__ ws, const u16* __restrict__ dskip,
                                              u16* __restrict__ outT) {
  __shared__ __attribute__((aligned(16))) u32x4 sm[CV_TOTAL];
  const int tg = threadIdx.x;
  const int cgp = __builtin_amdgcn_readfirstlane(tg >> 8), t = tg & 255, lane = t & 63;
  const int w = __builtin_amdgcn_readfirstlane(t >> 6);
  const int dreal = blockIdx.x * 3 + cgp;
  const bool valid = dreal < D_;
  const int d = valid ? dreal : D_ - 1;
  const int ci = lane & 31, h = lane >> 5;
  u32x4* cb = sm + cgp * CV_CH;
  const u32x4* kg = (const u32x4*)(ws + OFF_KF + (size_t)d * KFROW * 2);
  const u32x4 kc = kg[31 + t], kp = kg[30 + t];
  f32x2 sp[3][4];
  u32x4 sf0, sf1;
  if (FINAL) {
    sf0 = ((const u32x4*)st)[2 * t];
    sf1 = ((const u32x4*)st)[2 * t + 1];
  } else {
#pragma unroll
    for (int i = 0; i < 3; i++) {
      const int row = min(tg + 768 * i, L_ - 1);
#pragma unroll
      for (int p = 0; p < 4; p++) sp[i][p] = st[p * L_ + row];
    }
  }
  const u32x4 xv = *(const u32x4*)(xT + (size_t)d * L_ + 8 * t);
  const float kinv = ((const float*)(ws + OFF_KINV))[d];
  const int lo0 = 8 * ci + 4 * h + 256 * w;
  const u32x2 xo0 = *(const u32x2*)(xT + (size_t)d * L_ + lo0);
  const u32x2 xo1 = *(const u32x2*)(xT + (size_t)d * L_ + lo0 + 1024);
  const float dsk = bf2f(dskip[d]);
  {
    u32x4 z = {0, 0, 0, 0};
    if (tg < 48) sm[CV_ZERO + tg] = z;
    if (t < 32) cb[CV_UOFF + (t < 31 ? t : 287)] = z;
  }
  {
    unsigned Z[8], Y[7];
#pragma unroll
    for (int m = 0; m < 4; m++) {
      Z[m] = __builtin_amdgcn_alignbit(kc[3 - m], kc[3 - m], 16);
      Z[4 + m] = __builtin_amdgcn_alignbit(kp[3 - m], kp[3 - m], 16);
    }
#pragma unroll
    for (int m = 0; m < 7; m++) Y[m] = __builtin_amdgcn_alignbit(Z[m + 1], Z[m], 16);
    const int phys = (t >> 5) * 40 + (t & 31);
#pragma unroll
    for (int r = 0; r < 8; r++) {
      const int o = 7 - r;
      u32x4 v;
      if ((o & 1) == 0) { v[0] = Z[o / 2]; v[1] = Z[o / 2 + 1]; v[2] = Z[o / 2 + 2]; v[3] = Z[o / 2 + 3]; }
      else { v[0] = Y[o / 2]; v[1] = Y[o / 2 + 1]; v[2] = Y[o / 2 + 2]; v[3] = Y[o / 2 + 3]; }
      cb[r * CV_COPY + phys] = v;
    }
  }
  if (!FINAL) {
#pragma unroll
  for (int i = 0; i < 3; i++) {
    const int row = tg + 768 * i;
    float m0 = sp[i][0][0], m1 = sp[i][1][0], m2 = sp[i][2][0], m3 = sp[i][3][0];
    float mean = 0.25f * ((m0 + m1) + (m2 + m3));
    float a0 = m0 - mean, a1 = m1 - mean, a2 = m2 - mean, a3 = m3 - mean;
    float M2 = ((sp[i][0][1] + sp[i][1][1]) + (sp[i][2][1] + sp[i][3][1])) +
               160.0f * ((a0 * a0 + a1 * a1) + (a2 * a2 + a3 * a3));
    float vb = rbf(M2 * (1.0f / 640.0f));
    float sd = __builtin_amdgcn_sqrtf(rbf(vb + 1e-5f));
    if (row < L_) ((unsigned*)(sm + CV_STAT))[row] = pk2(mean, sd);
  }
  __syncthreads();
  }
  {
    const u32x4 s0 = FINAL ? sf0 : sm[CV_STAT + 2 * t], s1 = FINAL ? sf1 : sm[CV_STAT + 2 * t + 1];
    u32x4 c4;
#pragma unroll
    for (int i = 0; i < 4; i++) {
      const unsigned sa = i < 2 ? s0[2 * i] : s1[2 * i - 4], sb = i < 2 ? s0[2 * i + 1] : s1[2 * i - 3];
      float u0 = rbf(rbf(lo16(xv[i]) - lo16(sa)) * __builtin_amdgcn_rcpf(hi16(sa)));
      float u1 = rbf(rbf(hi16(xv[i]) - lo16(sb)) * __builtin_amdgcn_rcpf(hi16(sb)));
      _Float16 f0 = (_Float16)(u0 * 512.0f), f1 = (_Float16)(u1 * 512.0f);
      c4[i] = (unsigned)__builtin_bit_cast(u16, f0) | ((unsigned)__builtin_bit_cast(u16, f1) << 16);
    }
    cb[CV_UOFF + 31 + t] = c4;
  }
  __syncthreads();
  f32x16 T0 = {0}, T1 = {0};
  {
    const int r = ci & 7, al = ci >> 3;
    const int blo = al - w, bhi = al + 4 - w;
    const int alo = (blo >= 0) ? (cgp * CV_CH + r * CV_COPY + 40 * blo + 1 - h) : (CV_ZERO + r + 8 * (blo & 1) + 1 - h);
    const int ahi = cgp * CV_CH + r * CV_COPY + 40 * bhi + 1 - h;
    const int ua = cgp * CV_CH + CV_UOFF + ci + h + 32 * w;
    u32x4 a_lo = sm[alo + 30], a_hi = sm[ahi + 30], b_a = sm[ua], b_b = sm[ua + 128];
#pragma unroll
    for (int s = 0; s < 16; s++) {
      u32x4 n_lo, n_hi, n_a, n_b;
      if (s < 15) {
        n_lo = sm[alo + 28 - 2 * s]; n_hi = sm[ahi + 28 - 2 * s];
        n_a = sm[ua + 2 * s + 2]; n_b = sm[ua + 2 * s + 130];
      }
      __builtin_amdgcn_sched_barrier(0);
      T0 = __builtin_amdgcn_mfma_f32_32x32x16_f16(__builtin_bit_cast(f16x8, a_lo), __builtin_bit_cast(f16x8, b_a), T0, 0, 0, 0);
      T1 = __builtin_amdgcn_mfma_f32_32x32x16_f16(__builtin_bit_cast(f16x8, a_hi), __builtin_bit_cast(f16x8, b_a), T1, 0, 0, 0);
      T1 = __builtin_amdgcn_mfma_f32_32x32x16_f16(__builtin_bit_cast(f16x8, a_lo), __builtin_bit_cast(f16x8, b_b), T1, 0, 0, 0);
      __builtin_amdgcn_sched_barrier(0);
      if (s < 15) { a_lo = n_lo; a_hi = n_hi; b_a = n_a; b_b = n_b; }
    }
  }
  __syncthreads();
  f32x4* redc = (f32x4*)cb;
  f32x4 own[2];
#pragma unroll
  for (int g = 0; g < 4; g++) {
    f32x4 a = {T0[4 * g], T0[4 * g + 1], T0[4 * g + 2], T0[4 * g + 3]};
    f32x4 c = {T1[4 * g], T1[4 * g + 1], T1[4 * g + 2], T1[4 * g + 3]};
    if (g == w) { own[0] = a; own[1] = c; }
    else {
      if (g > w) redc[((w * 2 + 0) * 4 + g) * 64 + lane] = a;
      redc[((w * 2 + 1) * 4 + g) * 64 + lane] = c;
    }
  }
  __syncthreads();
#pragma unroll
  for (int T = 0; T < 2; T++) {
    f32x4 y = own[T];
#pragma unroll
    for (int ww = 0; ww < 4; ww++)
      if (ww != w && (T == 1 || ww < w)) y += redc[((ww * 2 + T) * 4 + w) * 64 + lane];
    const int l = lo0 + 1024 * T;
    const u32x2 ur = *((const u32x2*)(cb + CV_UOFF + 31 + (l >> 3)) + h);
    float uu[4];
    uu[0] = (float)__builtin_bit_cast(_Float16, (u16)(ur[0] & 0xffffu)) * (1.0f / 512.0f);
    uu[1] = (float)__builtin_bit_cast(_Float16, (u16)(ur[0] >> 16)) * (1.0f / 512.0f);
    uu[2] = (float)__builtin_bit_cast(_Float16, (u16)(ur[1] & 0xffffu)) * (1.0f / 512.0f);
    uu[3] = (float)__builtin_bit_cast(_Float16, (u16)(ur[1] >> 16)) * (1.0f / 512.0f);
    const u32x2 xo = T ? xo1 : xo0;
    float xx[4] = {lo16(xo[0]), hi16(xo[0]), lo16(xo[1]), hi16(xo[1])};
    float r[4];
#pragma unroll
    for (int e = 0; e < 4; e++) {
      float z = rbf(rbf(y[e] * kinv) + rbf(uu[e] * dsk));
      r[e] = xx[e] + z;
    }
    u32x2 o = {pk2(r[0], r[1]), pk2(r[2], r[3])};
    if (valid) *(u32x2*)(outT + (size_t)d * L_ + l) = o;
  }
}

template <int MODE>
__global__ __launch_bounds__(MODE == 0 ? 704 : 640) void k_gemm(const u16* __restrict__ XT, const u32x4* __restrict__ Wp,
                                                                const u16* __restrict__ resT, u16* __restrict__ out,
                                                                char* __restrict__ ws, f32x2* __restrict__ stout) {
  constexpr int NT = (MODE == 0) ? 11 : 10;
  constexpr int NTH = NT * 64;
  __shared__ __attribute__((aligned(16))) char smem[40960 + 32 * 164 * 4];
  const int t = threadIdx.x, lane = t & 63, w = __builtin_amdgcn_readfirstlane(t >> 6);
  const int b = blockIdx.x;
  const int cg = (b >> 3) & 3, rb = (b & 7) + 8 * (b >> 5);
  const int l0 = rb * 32;
  const int g = lane >> 4, li = lane & 15;
  const int ntile = cg * NT + w;
  u32x4 xv[4];
#pragma unroll
  for (int i = 0; i < 4; i++) {
    const int c = t + NTH * i;
    if (c < 2560) xv[i] = *(const u32x4*)(XT + (size_t)(c >> 2) * L_ + l0 + (c & 3) * 8);
  }
  const u32x4* wp0 = Wp + (size_t)ntile * 20 * 64 + lane;
  u32x4 wf[20];
#pragma unroll
  for (int p = 0; p < 3; p++) wf[p] = wp0[p * 64];
  __builtin_amdgcn_sched_barrier(0);
#pragma unroll
  for (int i = 0; i < 4; i++) {
    const int c = t + NTH * i;
    const int row = c >> 2;
    if (c < 2560) *(u32x4*)(smem + row * 64 + (((c & 3) ^ (2 * ((row >> 3) & 1))) * 16)) = xv[i];
  }
  __syncthreads();
  f32x4 acc[2] = {{0, 0, 0, 0}, {0, 0, 0, 0}};
  const int q4 = li >> 2, p4 = li & 3;
  const unsigned sbase = (unsigned)(size_t)smem;
  unsigned xa[2];
#pragma unroll
  for (int lt = 0; lt < 2; lt++)
    xa[lt] = sbase + (8 * g + q4) * 64 + (((2 * lt + (p4 >> 1)) ^ (2 * (g & 1))) * 16) + 8 * (p4 & 1);
  f32x2* stp = (f32x2*)(smem + 40960);
  if (MODE == 0) {
#pragma unroll
    for (int p = 3; p < 9; p++) wf[p] = wp0[p * 64];
    __builtin_amdgcn_sched_barrier(0);
    float ssum[2] = {0.f, 0.f}, ssq[2] = {0.f, 0.f};
    const bf16x2 ones = __builtin_bit_cast(bf16x2, 0x3F803F80u);
#pragma unroll
    for (int kk = 0; kk < 2; kk++) {
      const int ks = w + NT * kk;
      if (ks < 20) {
#pragma unroll
        for (int lt = 0; lt < 2; lt++) {
          const u32x4 xf = tr_frag(xa[lt] + ks * 2048, xa[lt] + ks * 2048 + 256);
#pragma unroll
          for (int e = 0; e < 4; e++) {
            const unsigned ux = xf[e];
            bf16x2 xx = __builtin_bit_cast(bf16x2, ux);
            ssum[lt] = __builtin_amdgcn_fdot2_f32_bf16(xx, ones, ssum[lt], false);
            ssq[lt] = __builtin_amdgcn_fdot2_f32_bf16(xx, xx, ssq[lt], false);
          }
        }
      }
    }
#pragma unroll
    for (int lt = 0; lt < 2; lt++) {
      ssum[lt] += __shfl_xor(ssum[lt], 16); ssum[lt] += __shfl_xor(ssum[lt], 32);
      ssq[lt] += __shfl_xor(ssq[lt], 16); ssq[lt] += __shfl_xor(ssq[lt], 32);
      if (g == 0) { f32x2 o = {ssum[lt], ssq[lt]}; stp[w * 32 + 16 * lt + li] = o; }
    }
  }
  __builtin_amdgcn_sched_barrier(0);
#pragma unroll
  for (int p = (MODE == 0) ? 9 : 3; p < 20; p++) wf[p] = wp0[p * 64];
  f32x4 c4;
  u32x2 xr[2];
  u16 xs[2][4];
  if (MODE == 0) c4 = *(const f32x4*)((const float*)(ws + OFF_CS) + ntile * 16 + 4 * g);
  if (MODE == 1) {
#pragma unroll
    for (int lt = 0; lt < 2; lt++) xr[lt] = *(const u32x2*)(resT + (size_t)(ntile * 16 + li) * L_ + l0 + 16 * lt + 4 * g);
  }
  if (MODE == 2) {
#pragma unroll
    for (int lt = 0; lt < 2; lt++)
#pragma unroll
      for (int e = 0; e < 4; e++) xs[lt][e] = resT[(size_t)(ntile * 16 + 4 * g + e) * L_ + l0 + 16 * lt + li];
  }
#pragma unroll
  for (int ks = 0; ks < 20; ks++) {
    u32x4 xf[2];
#pragma unroll
    for (int lt = 0; lt < 2; lt++) xf[lt] = tr_frag(xa[lt] + ks * 2048, xa[lt] + ks * 2048 + 256);
    const bf16x8 wv = __builtin_bit_cast(bf16x8, wf[ks]);
#pragma unroll
    for (int lt = 0; lt < 2; lt++) {
      if (MODE == 1) acc[lt] = __builtin_amdgcn_mfma_f32_16x16x32_bf16(__builtin_bit_cast(bf16x8, xf[lt]), wv, acc[lt], 0, 0, 0);
      else acc[lt] = __builtin_amdgcn_mfma_f32_16x16x32_bf16(wv, __builtin_bit_cast(bf16x8, xf[lt]), acc[lt], 0, 0, 0);
    }
  }
  if (MODE == 0) {
    __syncthreads();
    f32x2 svv[2];
#pragma unroll
    for (int lt = 0; lt < 2; lt++) {
      f32x2 a = {0.f, 0.f};
#pragma unroll
      for (int i = 0; i < 11; i++) a += stp[i * 32 + 16 * lt + li];
      float mean = a[0] * (1.0f / 640.0f);
      float var = fmaxf(a[1] * (1.0f / 640.0f) - mean * mean, 0.f);
      float sd = rbf(__builtin_amdgcn_sqrtf(rbf(rbf(var) + 1e-5f)));
      svv[lt] = (f32x2){rbf(mean), __builtin_amdgcn_rcpf(sd)};
    }
    u16* qo = (u16*)(ws + OFF_Q);
    char* kvimg = ws + OFF_KV;
    const int n0 = ntile * 16 + 4 * g;
#pragma unroll
    for (int lt = 0; lt < 2; lt++) {
      const f32x2 sv = svv[lt];
      const int l = l0 + 16 * lt + li;
      float v[4];
#pragma unroll
      for (int r = 0; r < 4; r++) v[r] = (acc[lt][r] - sv[0] * c4[r]) * sv[1];
      if (n0 < 640) {
        u32x2 o = {pk2(v[0] * QSCALE, v[1] * QSCALE), pk2(v[2] * QSCALE, v[3] * QSCALE)};
        *(u32x2*)(qo + (size_t)l * D_ + n0) = o;
      } else if (n0 < 660) {
        const int dh = n0 - 640;
        u32x2 o = {pk2(v[0], v[1]), pk2(v[2], v[3])};
        const int tile = l >> 5, ln = (l & 31) + 32 * ((dh & 15) >> 3);
        if (dh < 16) *(u32x2*)(kvimg + tile * KV_TB + ln * 16 + (dh & 7) * 2) = o;
        else *(u32x2*)(kvimg + tile * KV_TB + KV_K1 + (l & 31) * 8) = o;
      } else if (n0 < 680) {
        const int dv = n0 - 660;
        const int tile = l >> 5, kk = l & 15, step = (l & 31) >> 4;
        const int hh = (kk & 7) >> 2, jj = (kk >> 3) * 4 + (kk & 3);
#pragma unroll
        for (int r = 0; r < 4; r++)
          *(u16*)(kvimg + tile * KV_TB + KV_V0 + step * 672 + hh * 336 + (dv + r) * 16 + jj * 2) = f2bf(v[r]);
      }
    }
  } else if (MODE == 1) {
    float* stg = (float*)(smem + 40960);
    const int nl = w * 16 + li;
    const int n = cg * 160 + nl;
#pragma unroll
    for (int lt = 0; lt < 2; lt++) {
      const int ll = 16 * lt + 4 * g;
      float r[4];
      r[0] = rbf(lo16(xr[lt][0]) + rbf(acc[lt][0]));
      r[1] = rbf(hi16(xr[lt][0]) + rbf(acc[lt][1]));
      r[2] = rbf(lo16(xr[lt][1]) + rbf(acc[lt][2]));
      r[3] = rbf(hi16(xr[lt][1]) + rbf(acc[lt][3]));
      u32x2 o = {pk2(r[0], r[1]), pk2(r[2], r[3])};
      *(u32x2*)(out + (size_t)n * L_ + l0 + ll) = o;
#pragma unroll
      for (int e = 0; e < 4; e++) stg[(ll + e) * 164 + nl] = r[e];
    }
    __syncthreads();
    if (t < 256) {
      const int row = t >> 3, part = t & 7;
      float v[20];
      float s = 0.f;
#pragma unroll
      for (int i = 0; i < 5; i++) {
        f32x4 a = *(const f32x4*)(stg + row * 164 + part * 20 + 4 * i);
#pragma unroll
        for (int e = 0; e < 4; e++) { v[4 * i + e] = a[e]; s += a[e]; }
      }
      s += __shfl_xor(s, 1); s += __shfl_xor(s, 2); s += __shfl_xor(s, 4);
      const float mean = s * (1.0f / 160.0f);
      float m2 = 0.f;
#pragma unroll
      for (int i = 0; i < 20; i++) { float a = v[i] - mean; m2 += a * a; }
      m2 += __shfl_xor(m2, 1); m2 += __shfl_xor(m2, 2); m2 += __shfl_xor(m2, 4);
      if (part == 0) { f32x2 o = {mean, m2}; stout[cg * L_ + l0 + row] = o; }
    }
  } else {
    const int n0 = ntile * 16 + 4 * g;
#pragma unroll
    for (int lt = 0; lt < 2; lt++) {
      const int l = l0 + 16 * lt + li;
      float r[4];
#pragma unroll
      for (int e = 0; e < 4; e++) r[e] = bf2f(xs[lt][e]) + rbf(acc[lt][e]);
      u32x2 o = {pk2(r[0], r[1]), pk2(r[2], r[3])};
      *(u32x2*)(out + (size_t)l * D_ + n0) = o;
    }
  }
}

__device__ __forceinline__ void pack_p(const f32x16& s, bf16x8& p0, bf16x8& p1) {
  u32x4 a, c;
#pragma unroll
  for (int i = 0; i < 4; i++) {
    a[i] = pk2(__builtin_amdgcn_exp2f(s[2 * i]), __builtin_amdgcn_exp2f(s[2 * i + 1]));
    c[i] = pk2(__builtin_amdgcn_exp2f(s[8 + 2 * i]), __builtin_amdgcn_exp2f(s[9 + 2 * i]));
  }
  p0 = __builtin_bit_cast(bf16x8, a);
  p1 = __builtin_bit_cast(bf16x8, c);
}
__device__ __forceinline__ float max16(const f32x16& s) {
  float m = fmaxf(s[0], s[1]);
#pragma unroll
  for (int i = 2; i < 16; i++) m = fmaxf(m, s[i]);
  return m;
}

__device__ __forceinline__ u32x4 k1frag(u32x2 v) { return (u32x4){v[0], v[1], 0u, 0u}; }

__global__ __launch_bounds__(1024) void k_attn(const char* __restrict__ ws_q, const char* __restrict__ kv,
                                               u16* __restrict__ oT) {
  __shared__ __attribute__((aligned(16))) char sm[62 * KV_TB];
  const int t = threadIdx.x, lane = t & 63, w = __builtin_amdgcn_readfirstlane(t >> 6);
  const int b = blockIdx.x;
  const int kh = w >> 3;
  const int pb = (b & 7) + 8 * (b >> 5), head = ((b >> 3) & 3) * 8 + (w & 7);
  const int ci = lane & 31, h = lane >> 5;
  const int l = pb * 32 + ci;
#pragma unroll
  for (int hh = 0; hh < 2; hh++)
    for (int i = w * 64; i < 8 * 164; i += 1024)
      if (i + lane < 8 * 164)
        __builtin_amdgcn_global_load_lds((const __attribute__((address_space(1))) void*)(kv + (size_t)hh * 32 * KV_TB + (i + lane) * 16),
                                         (LDS_AS void*)(sm + hh * 31 * KV_TB + (i + lane) * 16), 16, 0, 0);
  bf16x8 q0, q1;
  s16x4 q1s;
  {
    const char* qp = ws_q + (size_t)l * (D_ * 2) + head * 40;
    u32x2 a = *(const u32x2*)(qp + 16 * h), c = *(const u32x2*)(qp + 16 * h + 8);
    u32x2 e = *(const u32x2*)(qp + 32);
    u32x4 f0 = {a[0], a[1], c[0], c[1]};
    u32x4 f1 = {h ? 0u : e[0], h ? 0u : e[1], 0u, 0u};
    q0 = __builtin_bit_cast(bf16x8, f0);
    q1 = __builtin_bit_cast(bf16x8, f1);
    u32x2 f1s = {f1[0], f1[1]};
    q1s = __builtin_bit_cast(s16x4, f1s);
  }
  __syncthreads();
#define DMA_B(j)                                                                                                      \
  {                                                                                                                   \
    const int hh_ = (j) & 1, i_ = 8 * 164 + w * 64 + ((j) >> 1) * 1024;                                               \
    if (i_ + lane < 31 * 164)                                                                                         \
      __builtin_amdgcn_global_load_lds((const __attribute__((address_space(1))) void*)(kv + (size_t)hh_ * 32 * KV_TB + (i_ + lane) * 16), \
                                       (LDS_AS void*)(sm + hh_ * 31 * KV_TB + (i_ + lane) * 16), 16, 0, 0);          \
  }
  DMA_B(0) DMA_B(1)
  const int ok0 = lane * 16, ok1 = KV_K1 + ci * 8, ov0 = KV_V0 + h * 336 + min(ci, 20) * 16, ov1 = ov0 + 672;
  f32x16 negm;
  {
    f32x16 z = {0};
    f32x16 s = __builtin_amdgcn_mfma_f32_32x32x16_bf16(*(const bf16x8*)(sm + ok0), q0, z, 0, 0, 0);
    s = __builtin_amdgcn_mfma_f32_32x32x16_bf16(__builtin_bit_cast(bf16x8, k1frag(*(const u32x2*)(sm + ok1))), q1, s, 0, 0, 0);
    float m = max16(s);
    m = fmaxf(m, __shfl_xor(m, 32));
#pragma unroll
    for (int i = 0; i < 16; i++) negm[i] = -m;
  }
  f32x16 o = {0};
#define SB() __builtin_amdgcn_sched_barrier(0)
  const char* base = sm + kh * 31 * KV_TB;
  const char* gl = kv + (size_t)(kh * 32 + 31) * KV_TB;
  u32x4 k0n = *(const u32x4*)(base + ok0);
  u32x2 k1n = *(const u32x2*)(base + ok1);
  u32x4 v0 = *(const u32x4*)(base + ov0), v1 = *(const u32x4*)(base + ov1);
  f32x16 s = __builtin_amdgcn_mfma_f32_32x32x16_bf16(__builtin_bit_cast(bf16x8, k0n), q0, negm, 0, 0, 0);
  s = __builtin_amdgcn_mfma_f32_32x32x16_bf16(__builtin_bit_cast(bf16x8, k1frag(k1n)), q1, s, 0, 0, 0);
  k0n = *(const u32x4*)(base + KV_TB + ok0);
  k1n = *(const u32x2*)(base + KV_TB + ok1);
#define TILE(KP, VP, NEXT)                                                                                            \
  {                                                                                                                   \
    u32x4 pa, pb;                                                                                                     \
    f32x16 sn;                                                                                                        \
    if (NEXT) sn = __builtin_amdgcn_mfma_f32_32x32x16_bf16(__builtin_bit_cast(bf16x8, k0n), q0, negm, 0, 0, 0);       \
    SB();                                                                                                             \
    pa[0] = pk2(__builtin_amdgcn_exp2f(s[0]), __builtin_amdgcn_exp2f(s[1]));                                          \
    pa[1] = pk2(__builtin_amdgcn_exp2f(s[2]), __builtin_amdgcn_exp2f(s[3]));                                          \
    SB();                                                                                                             \
    if (NEXT) sn = __builtin_amdgcn_mfma_f32_32x32x8bf16_1k(__builtin_bit_cast(s16x4, k1n), q1s, sn, 0, 0, 0);       \
    u32x4 v0n = v0, v1n = v1;                                                                                         \
    if (NEXT) { k0n = *(const u32x4*)((KP) + ok0); k1n = *(const u32x2*)((KP) + ok1); }                               \
    if (NEXT) { v0n = *(const u32x4*)((VP) + ov0); v1n = *(const u32x4*)((VP) + ov1); }                               \
    SB();                                                                                                             \
    pa[2] = pk2(__builtin_amdgcn_exp2f(s[4]), __builtin_amdgcn_exp2f(s[5]));                                          \
    pa[3] = pk2(__builtin_amdgcn_exp2f(s[6]), __builtin_amdgcn_exp2f(s[7]));                                          \
    pb[0] = pk2(__builtin_amdgcn_exp2f(s[8]), __builtin_amdgcn_exp2f(s[9]));                                          \
    SB();                                                                                                             \
    o = __builtin_amdgcn_mfma_f32_32x32x16_bf16(__builtin_bit_cast(bf16x8, v0), __builtin_bit_cast(bf16x8, pa), o, 0, 0, 0); \
    SB();                                                                                                             \
    pb[1] = pk2(__builtin_amdgcn_exp2f(s[10]), __builtin_amdgcn_exp2f(s[11]));                                        \
    pb[2] = pk2(__builtin_amdgcn_exp2f(s[12]), __builtin_amdgcn_exp2f(s[13]));                                        \
    pb[3] = pk2(__builtin_amdgcn_exp2f(s[14]), __builtin_amdgcn_exp2f(s[15]));                                        \
    SB();                                                                                                             \
    o = __builtin_amdgcn_mfma_f32_32x32x16_bf16(__builtin_bit_cast(bf16x8, v1), __builtin_bit_cast(bf16x8, pb), o, 0, 0, 0); \
    v0 = v0n; v1 = v1n;                                                                                               \
    if (NEXT) s = sn;                                                                                                 \
    SB();                                                                                                             \
  }
  __builtin_amdgcn_s_setprio(3);
#pragma unroll
  for (int tl = 0; tl < 6; tl++) {
    if (tl < 3) { DMA_B(2 * tl + 2) DMA_B(2 * tl + 3) }
    TILE(base + (tl + 2) * KV_TB, base + (tl + 1) * KV_TB, true)
  }
  __syncthreads();
#pragma unroll 2
  for (int tl = 6; tl < 20; tl++) TILE(base + (tl + 2) * KV_TB, base + (tl + 1) * KV_TB, true)
  __builtin_amdgcn_s_setprio(2);
#pragma unroll 2
  for (int tl = 20; tl < 28; tl++) TILE(base + (tl + 2) * KV_TB, base + (tl + 1) * KV_TB, true)
  __builtin_amdgcn_s_setprio(1);
  TILE(base + 30 * KV_TB, base + 29 * KV_TB, true)
  TILE(gl, base + 30 * KV_TB, true)
  TILE(gl, gl, true)
  __builtin_amdgcn_s_setprio(0);
  TILE(gl, gl, false)
  __syncthreads();
  f32x4* mg = (f32x4*)sm;
  if (kh == 1) {
#pragma unroll
    for (int i = 0; i < 3; i++) {
      f32x4 a = {o[4 * i], o[4 * i + 1], o[4 * i + 2], o[4 * i + 3]};
      mg[((w & 7) * 3 + i) * 64 + lane] = a;
    }
  }
  __syncthreads();
  if (kh == 1) return;
#pragma unroll
  for (int i = 0; i < 3; i++) {
    f32x4 a = mg[(w * 3 + i) * 64 + lane];
#pragma unroll
    for (int e = 0; e < 4; e++) o[4 * i + e] += a[e];
  }
  float lsum = __shfl(o[8], ci + 32);
  bool bad = !(lsum <= 1.8446744e19f);
#pragma unroll
  for (int i = 0; i < 12; i++) bad = bad || !(fabsf(o[i]) <= 3.0e38f);
  if (__any(bad)) {
    float m = -INFINITY;
#pragma unroll
    for (int i = 0; i < 16; i++) o[i] = 0.f;
    for (int tl = 0; tl < 64; tl++) {
      const char* tp = kv + (size_t)tl * KV_TB;
      f32x16 z = {0};
      f32x16 s2 = __builtin_amdgcn_mfma_f32_32x32x16_bf16(*(const bf16x8*)(tp + ok0), q0, z, 0, 0, 0);
      s2 = __builtin_amdgcn_mfma_f32_32x32x16_bf16(__builtin_bit_cast(bf16x8, k1frag(*(const u32x2*)(tp + ok1))), q1, s2, 0, 0, 0);
      float tm = max16(s2);
      tm = fmaxf(tm, __shfl_xor(tm, 32));
      const float mn = fmaxf(m, tm);
      const float al = __builtin_amdgcn_exp2f(m - mn);
      m = mn;
#pragma unroll
      for (int i = 0; i < 16; i++) { o[i] *= al; s2[i] -= mn; }
      bf16x8 p0, p1;
      pack_p(s2, p0, p1);
      o = __builtin_amdgcn_mfma_f32_32x32x16_bf16(*(const bf16x8*)(tp + ov0), p0, o, 0, 0, 0);
      o = __builtin_amdgcn_mfma_f32_32x32x16_bf16(*(const bf16x8*)(tp + ov1), p1, o, 0, 0, 0);
    }
    lsum = __shfl(o[8], ci + 32);
  }
  const float inv = 1.0f / lsum;
  u16* op = oT + (size_t)(head * 20 + 4 * h) * L_ + l;
#pragma unroll
  for (int i = 0; i < 12; i++) {
    const int dd = (i & 3) + 8 * (i >> 2);
    if (i < 8 || h == 0) op[(size_t)dd * L_] = f2bf(o[i] * inv);
  }
}

extern "C" void kernel_launch(void* const* d_in, const int* in_sizes, int n_in, void* d_out, int out_size,
                              void* d_ws, size_t ws_size, hipStream_t stream) {
  const u16* x = (const u16*)d_in[0];
  const u16* logA = (const u16*)d_in[1];
  const u16* Aim = (const u16*)d_in[2];
  const u16* Cre = (const u16*)d_in[3];
  const u16* Cim = (const u16*)d_in[4];
  const u16* logdt = (const u16*)d_in[5];
  const u16* dskip = (const u16*)d_in[6];
  const u16* wq = (const u16*)d_in[8];
  const u16* wk = (const u16*)d_in[9];
  const u16* wv = (const u16*)d_in[10];
  const u16* wo = (const u16*)d_in[11];
  char* ws = (char*)d_ws;
  u16* xt0 = (u16*)(ws + OFF_XT0);
  u16* x1t = (u16*)(ws + OFF_X1T);
  u16* x2t = (u16*)(ws + OFF_X2T);
  u16* x3t = (u16*)(ws + OFF_X3T);
  u16* ot = (u16*)(ws + OFF_OT);
  const u32x4* wqkv = (const u32x4*)(ws + OFF_WQKV);
  const u32x4* wop = (const u32x4*)(ws + OFF_WO);
  const char* kv = ws + OFF_KV;
  f32x2* st0 = (f32x2*)(ws + OFF_ST0);
  f32x2* st1 = (f32x2*)(ws + OFF_ST1);

  k_prep<<<PREP_WGS, 1024, 0, stream>>>(x, logA, Aim, Cre, Cim, logdt, wq, wk, wv, wo, ws);
  k_conv<true><<<214, 768, 0, stream>>>(xt0, st0, ws, dskip, x1t);
  k_gemm<0><<<256, 704, 0, stream>>>(x1t, wqkv, nullptr, nullptr, ws, nullptr);
  k_attn<<<256, 1024, 0, stream>>>(ws + OFF_Q, kv, ot);
  k_gemm<1><<<256, 640, 0, stream>>>(ot, wop, x1t, x2t, ws, st1);
  k_conv<false><<<214, 768, 0, stream>>>(x2t, st1, ws, dskip, x3t);
  k_gemm<0><<<256, 704, 0, stream>>>(x3t, wqkv, nullptr, nullptr, ws, nullptr);
  k_attn<<<256, 1024, 0, stream>>>(ws + OFF_Q, kv, ot);
  k_gemm<2><<<256, 640, 0, stream>>>(ot, wop, x3t, (u16*)d_out, ws, nullptr);
}
```
